```python
import torch

def _softmax(x, axis=-1):
    return torch.softmax(x, dim=axis)
def _gelu(x):
    return torch.nn.functional.gelu(x, approximate="tanh")
def _silu(x):
    return torch.nn.functional.silu(x)
def _sigmoid(x):
    return torch.sigmoid(x)
def _layernorm(x, axis=-1, eps=1e-5):
    m = x.mean(dim=axis, keepdim=True)
    v = x.var(dim=axis, keepdim=True, unbiased=False)
    return (x - m) / torch.sqrt(v + eps)
def _scatter_add(tgt, idx, src, axis=0):
    return tgt.clone().index_add_(axis, idx, src)
def _segment_sum(src, seg, n):
    out = torch.zeros((n,) + src.shape[1:], dtype=src.dtype)
    return out.index_add_(0, seg, src)
def _pad(x, pw):
    flat = tuple(v for pair in reversed(pw) for v in pair)
    return torch.nn.functional.pad(x, flat)

kernel_name = 'interleave_layers__grouped_kv_mqa__dense_activation_relu__prompt'

def setup_inputs():

    _w = (lambda a: (lambda t: t.to(torch.bfloat16) if t.is_floating_point() else t)(torch.as_tensor(a)))
    def _A_setup():
        B, S, D, H = 1, 2048, 512, 32
        Dh = D // H
        return {
            "x": _w((lambda *s: torch.randn(*s, dtype=torch.bfloat16) * 0.05)(B, S, D)),
            "wq": _w((lambda *s: torch.randn(*s, dtype=torch.bfloat16) * 0.05)(D, H * Dh)),
            "wk": _w((lambda *s: torch.randn(*s, dtype=torch.bfloat16) * 0.05)(D, Dh)),
            "wv": _w((lambda *s: torch.randn(*s, dtype=torch.bfloat16) * 0.05)(D, Dh)),
            "wo": _w((lambda *s: torch.randn(*s, dtype=torch.bfloat16) * 0.05)(H * Dh, D)),
        }
    _A_in = _A_setup()

    def _B_setup():
        B, S, D, Dff = 1, 2048, 512, 1792
        return {
            "x": _w((lambda *s: torch.randn(*s, dtype=torch.bfloat16) * 0.05)(B, S, D)),
            "w1": _w((lambda *s: torch.randn(*s, dtype=torch.bfloat16) * 0.05)(D, Dff)),
            "w2": _w((lambda *s: torch.randn(*s, dtype=torch.bfloat16) * 0.05)(Dff, D)),
        }
    _B_in = _B_setup()

    return {
        "A_x": _A_in["x"],
        "A_wq": _A_in["wq"],
        "A_wk": _A_in["wk"],
        "A_wv": _A_in["wv"],
        "A_wo": _A_in["wo"],
        "B_x": _B_in["x"],
        "B_w1": _B_in["w1"],
        "B_w2": _B_in["w2"],
    }

def reference(A_x, A_wq, A_wk, A_wv, A_wo, B_x, B_w1, B_w2):
    def _A_ref(x, wq, wk, wv, wo):
        B, S, D = x.shape
        H = 32
        Dh = D // H
        q = (lambda x, *d: x.permute(*d))((x @ wq).reshape(B, S, H, Dh), 0, 2, 1, 3)
        k = x @ wk
        v = x @ wv
        scores = torch.einsum("bhqd,bkd->bhqk", q, k) * (Dh ** -0.5)
        p = _softmax(scores, axis=-1)
        o = torch.einsum("bhqk,bkd->bhqd", p, v)
        return (lambda x, *d: x.permute(*d))(o, 0, 2, 1, 3).reshape(B, S, H * Dh) @ wo

    def _B_ref(x, w1, w2):
        h = torch.einsum("bsd,df->bsf", x, w1)
        zero = torch.zeros_like(h)
        h = torch.maximum(h, zero)
        return torch.einsum("bsf,fd->bsd", h, w2)

    x = A_x
    x = x + _A_ref(_layernorm(x, axis=-1), A_wq, A_wk, A_wv, A_wo)
    x = x + _B_ref(_layernorm(x, axis=-1), B_w1, B_w2)
    x = x + _A_ref(_layernorm(x, axis=-1), A_wq, A_wk, A_wv, A_wo)
    x = x + _B_ref(_layernorm(x, axis=-1), B_w1, B_w2)
    return x
```

```cpp
#include <hip/hip_runtime.h>
#include <hip/hip_bf16.h>
#include <stdint.h>

typedef __attribute__((ext_vector_type(8))) __bf16 bf16x8;
typedef __attribute__((ext_vector_type(2))) __bf16 bf16x2;
typedef __attribute__((ext_vector_type(16))) float f32x16;
typedef __attribute__((ext_vector_type(4))) float f32x4;
typedef __attribute__((ext_vector_type(2))) float f32x2;
typedef __attribute__((ext_vector_type(4))) unsigned u32x4;
typedef __attribute__((ext_vector_type(2))) unsigned u32x2;
#define DI __device__ __forceinline__
#define MFMA(a, b, c) __builtin_amdgcn_mfma_f32_32x32x16_bf16(__builtin_bit_cast(bf16x8, (a)), __builtin_bit_cast(bf16x8, (b)), (c), 0, 0, 0)

constexpr int S_ = 2048, D_ = 512, H_ = 32, DFF = 1792;
constexpr int NTT = S_ / 32;
constexpr int KS_D = D_ / 16;
constexpr int KS_F = DFF / 16;
constexpr int VBLK = 544;
constexpr int KIMG_BYTES = NTT * 1024;
constexpr int VIMG_BYTES = NTT * 2 * VBLK;
constexpr float QSCALE = 0.25f * 1.4426950408889634f;

constexpr size_t WS_WQKV = 0;
constexpr size_t WS_WO   = WS_WQKV + 17 * 32 * 1024;
constexpr size_t WS_W1   = WS_WO + 16 * 32 * 1024;
constexpr size_t WS_W2   = WS_W1 + 56 * 32 * 1024;
constexpr size_t WS_CSQKV = WS_W2 + 16 * 112 * 1024;
constexpr size_t WS_CS1  = WS_CSQKV + 4096;
constexpr size_t WS_XA   = WS_CS1 + 8192;
constexpr size_t WS_XB   = WS_XA + 2 * 1024 * 1024;
constexpr size_t WS_STA  = WS_XB + 2 * 1024 * 1024;
constexpr size_t WS_STB  = WS_STA + 256 * 1024;
constexpr size_t WS_Q    = WS_STB + 256 * 1024;
constexpr size_t WS_KV   = WS_Q + 2 * 1024 * 1024;
constexpr size_t WS_O    = WS_KV + 256 * 1024;
constexpr size_t WS_H    = WS_O + 2 * 1024 * 1024;
constexpr size_t WS_END  = WS_H + (size_t)S_ * DFF * 2;

DI unsigned pk2(float a, float b) { f32x2 v = {a, b}; bf16x2 r = __builtin_convertvector(v, bf16x2); return __builtin_bit_cast(unsigned, r); }
DI float bflo(unsigned u) { return __uint_as_float(u << 16); }
DI float bfhi(unsigned u) { return __uint_as_float(u & 0xffff0000u); }
DI float xhalf(float v) {
  auto rr = __builtin_amdgcn_permlane32_swap(__float_as_uint(v), __float_as_uint(v), false, false);
  return (threadIdx.x & 32) ? __uint_as_float(rr[0]) : __uint_as_float(rr[1]);
}
DI float xsum(float v) {
  auto rr = __builtin_amdgcn_permlane32_swap(__float_as_uint(v), __float_as_uint(v), false, false);
  return __uint_as_float(rr[0]) + __uint_as_float(rr[1]);
}
DI float xmax(float v) {
  auto rr = __builtin_amdgcn_permlane32_swap(__float_as_uint(v), __float_as_uint(v), false, false);
  return __builtin_fmaxf(__uint_as_float(rr[0]), __uint_as_float(rr[1]));
}
DI int perm16(int h, int j) { return 8 * (j >> 2) + 4 * h + (j & 3); }

DI f32x2 tile_stats(const float* v) {
  float s = 0.f;
#pragma unroll
  for (int i = 0; i < 16; ++i) s += v[i];
  s = xsum(s);
  const float mu = s * (1.0f / 32.0f);
  float m2 = 0.f;
#pragma unroll
  for (int i = 0; i < 16; ++i) { const float d = v[i] - mu; m2 = __builtin_fmaf(d, d, m2); }
  m2 = xsum(m2);
  f32x2 r = {s, m2};
  return r;
}
DI f32x2 combine_stats(const f32x2* p) {
  float s = 0.f;
#pragma unroll
  for (int i = 0; i < 8; ++i) s += p[i][0];
  s = xsum(s);
  const float mean = s * (1.0f / 512.0f);
  float m2 = 0.f;
#pragma unroll
  for (int i = 0; i < 8; ++i) { const float d = p[i][0] * (1.0f / 32.0f) - mean; m2 += p[i][1] + 32.0f * d * d; }
  m2 = xsum(m2);
  const float var = m2 * (1.0f / 512.0f);
  f32x2 r = {mean, 1.0f / __builtin_sqrtf(var + 1e-5f)};
  return r;
}

DI void tile_weight(const unsigned short* W, int ldw, int col0, int s, char* dst, int lane) {
  const int r = lane & 31, h = lane >> 5;
  unsigned short e[8];
#pragma unroll
  for (int j = 0; j < 8; ++j) e[j] = W[(size_t)(16 * s + perm16(h, j)) * ldw + col0 + r];
  u32x4 o;
#pragma unroll
  for (int j = 0; j < 4; ++j) o[j] = (unsigned)e[2 * j] | ((unsigned)e[2 * j + 1] << 16);
  *(u32x4*)(dst + lane * 16) = o;
}

__global__ void __launch_bounds__(256) prep_kernel(const unsigned short* x, const unsigned short* wq, const unsigned short* wk,
                                                   const unsigned short* wv, const unsigned short* wo, const unsigned short* w1,
                                                   const unsigned short* w2, char* ws) {
  const int lane = threadIdx.x & 63, r = lane & 31, h = lane >> 5;
  const int gw = blockIdx.x * 4 + (threadIdx.x >> 6);
  const int nw = gridDim.x * 4;
  for (int job = gw; job < 5664 + 73; job += nw) {
    if (job < 512) { const int ft = job >> 5, s = job & 31; tile_weight(wq, 512, 32 * ft, s, ws + WS_WQKV + (size_t)job * 1024, lane); }
    else if (job < 544) {
      const int s = job - 512;
      unsigned short e[8];
      const unsigned short* W = r < 16 ? wk : wv;
#pragma unroll
      for (int j = 0; j < 8; ++j) e[j] = W[(16 * s + perm16(h, j)) * 16 + (r & 15)];
      u32x4 o;
#pragma unroll
      for (int j = 0; j < 4; ++j) o[j] = (unsigned)e[2 * j] | ((unsigned)e[2 * j + 1] << 16);
      *(u32x4*)(ws + WS_WQKV + (size_t)job * 1024 + lane * 16) = o;
    }
    else if (job < 1056) { const int jj = job - 544, ft = jj >> 5, s = jj & 31; tile_weight(wo, 512, 32 * ft, s, ws + WS_WO + (size_t)jj * 1024, lane); }
    else if (job < 2848) { const int jj = job - 1056, ft = jj >> 5, s = jj & 31; tile_weight(w1, DFF, 32 * ft, s, ws + WS_W1 + (size_t)jj * 1024, lane); }
    else if (job < 4640) { const int jj = job - 2848, ft = jj / 112, s = jj % 112; tile_weight(w2, 512, 32 * ft, s, ws + WS_W2 + (size_t)jj * 1024, lane); }
    else if (job < 5664) {
      const int jj = job - 4640, tt = jj >> 4, ft = jj & 15;
      const unsigned short* row = x + (size_t)(32 * tt + r) * 512 + 32 * ft + 4 * h;
      u32x2 a[4];
#pragma unroll
      for (int i = 0; i < 4; ++i) a[i] = *(const u32x2*)(row + 8 * i);
      float v[16];
#pragma unroll
      for (int i = 0; i < 4; ++i) { v[4 * i] = bflo(a[i][0]); v[4 * i + 1] = bfhi(a[i][0]); v[4 * i + 2] = bflo(a[i][1]); v[4 * i + 3] = bfhi(a[i][1]); }
      const f32x2 st = tile_stats(v);
      char* xt = ws + WS_XA + ((size_t)tt * KS_D + 2 * ft) * 1024 + lane * 16;
      u32x4 o0 = {a[0][0], a[0][1], a[1][0], a[1][1]}, o1 = {a[2][0], a[2][1], a[3][0], a[3][1]};
      *(u32x4*)xt = o0; *(u32x4*)(xt + 1024) = o1;
      if (h == 0) *(f32x2*)(ws + WS_STA + ((size_t)(tt * 16 + ft) * 32 + r) * 8) = st;
    }
    else {
      const int jj = job - 5664;
      const unsigned short* W; int ldw, col; float* dst;
      if (jj < 16) { W = wq; ldw = 512; col = 32 * jj + r; dst = (float*)(ws + WS_CSQKV) + 32 * jj + r; }
      else if (jj == 16) { W = r < 16 ? wk : wv; ldw = 16; col = r & 15; dst = (float*)(ws + WS_CSQKV) + 512 + r; }
      else { W = w1; ldw = DFF; col = 32 * (jj - 17) + r; dst = (float*)(ws + WS_CS1) + 32 * (jj - 17) + r; }
      float s = 0.f;
#pragma unroll 16
      for (int k = 0; k < 256; ++k) s += __uint_as_float((unsigned)W[(size_t)(256 * h + k) * ldw + col] << 16);
      s = xsum(s);
      if (h == 0) *dst = s;
    }
  }
}

template <int KS, int DEPTH>
DI void gemm64_core(const char* wt, const char* xt, f32x16 (&acc)[2][2], int lane) {
  constexpr int KW = KS / 4;
  u32x4 wf[DEPTH][2], xf[DEPTH][2];
#pragma unroll
  for (int i = 0; i < DEPTH && i < KW; ++i) {
    wf[i][0] = *(const u32x4*)(wt + i * 1024);
    xf[i][0] = *(const u32x4*)(xt + i * 1024);
    wf[i][1] = *(const u32x4*)(wt + (size_t)KS * 1024 + i * 1024);
    xf[i][1] = *(const u32x4*)(xt + (size_t)KS * 1024 + i * 1024);
  }
#pragma unroll
  for (int i = 0; i < KW; ++i) {
    const int b = i % DEPTH;
    const u32x4 w0 = wf[b][0], w1 = wf[b][1], x0 = xf[b][0], x1 = xf[b][1];
    acc[0][0] = MFMA(w0, x0, acc[0][0]);
    acc[1][0] = MFMA(w1, x0, acc[1][0]);
    acc[0][1] = MFMA(w0, x1, acc[0][1]);
    acc[1][1] = MFMA(w1, x1, acc[1][1]);
    if (i + DEPTH < KW) {
      const int n = i + DEPTH;
      wf[b][0] = *(const u32x4*)(wt + n * 1024);
      xf[b][0] = *(const u32x4*)(xt + n * 1024);
      wf[b][1] = *(const u32x4*)(wt + (size_t)KS * 1024 + n * 1024);
      xf[b][1] = *(const u32x4*)(xt + (size_t)KS * 1024 + n * 1024);
    }
  }
}

DI void reduce4(f32x16 (&acc)[2][2], float* lds, int w, int lane, f32x16& out) {
#pragma unroll
  for (int q = 0; q < 4; ++q) {
    if (q != w) {
      const f32x16 a = acc[q >> 1][q & 1];
#pragma unroll
      for (int g = 0; g < 4; ++g) {
        f32x4 v = {a[4 * g], a[4 * g + 1], a[4 * g + 2], a[4 * g + 3]};
        *(f32x4*)(lds + (((q * 4 + w) * 4 + g) * 64 + lane) * 4) = v;
      }
    }
  }
  __syncthreads();
  f32x16 o = (w == 0) ? acc[0][0] : (w == 1) ? acc[0][1] : (w == 2) ? acc[1][0] : acc[1][1];
#pragma unroll
  for (int sw = 0; sw < 4; ++sw) {
    if (sw != w) {
#pragma unroll
      for (int g = 0; g < 4; ++g) {
        const f32x4 v = *(const f32x4*)(lds + (((w * 4 + sw) * 4 + g) * 64 + lane) * 4);
        o[4 * g] += v[0]; o[4 * g + 1] += v[1]; o[4 * g + 2] += v[2]; o[4 * g + 3] += v[3];
      }
    }
  }
  out = o;
}

__global__ void __launch_bounds__(256) qkv_kernel(const char* xt_g, const char* st_g, char* ws) {
  __shared__ __attribute__((aligned(16))) float lds[16 * 4 * 64 * 4 + 2 * 4 * 4 * 64 * 4 + 64];
  const int lane = threadIdx.x & 63, r = lane & 31, h = lane >> 5;
  const int w = __builtin_amdgcn_readfirstlane(threadIdx.x >> 6);
  const int xcd = blockIdx.x & 7, idx = blockIdx.x >> 3;
  const int tp = 4 * xcd + (idx >> 3), fp = idx & 7;
  const int fi = w >> 1, ti = w & 1;
  const bool kv = fp < 2;
  f32x2 st[8];
  {
    const char* sp = st_g + ((size_t)((2 * tp + ti) * 16 + 8 * h) * 32 + r) * 8;
#pragma unroll
    for (int i = 0; i < 8; ++i) st[i] = *(const f32x2*)(sp + i * 256);
  }
  f32x4 cs[4];
  {
    const float* cp = (const float*)(ws + WS_CSQKV) + 32 * (2 * fp + fi) + 4 * h;
#pragma unroll
    for (int g = 0; g < 4; ++g) cs[g] = *(const f32x4*)(cp + 8 * g);
  }
  f32x16 acc[2][2] = {};
  const char* wt = ws + WS_WQKV + ((size_t)(2 * fp) * KS_D + 8 * w) * 1024 + lane * 16;
  const char* xt = xt_g + ((size_t)(2 * tp) * KS_D + 8 * w) * 1024 + lane * 16;
  gemm64_core<KS_D, 8>(wt, xt, acc, lane);
  f32x16 accK = {}, accV = {};
  if (kv) {
    const char* wkv = ws + WS_WQKV + ((size_t)16 * KS_D + 8 * w) * 1024 + lane * 16;
    const char* xk = xt + (size_t)fp * KS_D * 1024;
#pragma unroll
    for (int i = 0; i < 8; ++i) {
      const u32x4 a = *(const u32x4*)(wkv + i * 1024);
      const u32x4 b = *(const u32x4*)(xk + i * 1024);
      accK = MFMA(a, b, accK);
      accV = MFMA(b, a, accV);
    }
    float* l2 = lds + 16 * 4 * 64 * 4;
#pragma unroll
    for (int g = 0; g < 4; ++g) {
      f32x4 v = {accK[4 * g], accK[4 * g + 1], accK[4 * g + 2], accK[4 * g + 3]};
      *(f32x4*)(l2 + ((w * 4 + g) * 64 + lane) * 4) = v;
      f32x4 u = {accV[4 * g], accV[4 * g + 1], accV[4 * g + 2], accV[4 * g + 3]};
      *(f32x4*)(l2 + (((4 + w) * 4 + g) * 64 + lane) * 4) = u;
    }
  }
  const f32x2 ms = combine_stats(st);
  float* lms = lds + 16 * 4 * 64 * 4 + 2 * 4 * 4 * 64 * 4;
  if (kv && w == fp && h == 0) { lms[r] = ms[0]; lms[32 + r] = ms[1]; }
  f32x16 o;
  reduce4(acc, lds, w, lane, o);
  {
    const float nm = -ms[0], sc = ms[1] * QSCALE;
    u32x4 q0, q1;
#pragma unroll
    for (int g = 0; g < 4; ++g) {
      const float a = __builtin_fmaf(nm, cs[g][0], o[4 * g]) * sc, b = __builtin_fmaf(nm, cs[g][1], o[4 * g + 1]) * sc;
      const float c = __builtin_fmaf(nm, cs[g][2], o[4 * g + 2]) * sc, d = __builtin_fmaf(nm, cs[g][3], o[4 * g + 3]) * sc;
      if (g < 2) { q0[2 * g] = pk2(a, b); q0[2 * g + 1] = pk2(c, d); }
      else { q1[2 * (g - 2)] = pk2(a, b); q1[2 * (g - 2) + 1] = pk2(c, d); }
    }
    char* qp = ws + WS_Q + ((size_t)(2 * tp + ti) * 32 + 2 * (2 * fp + fi)) * 1024 + lane * 16;
    *(u32x4*)qp = q0; *(u32x4*)(qp + 1024) = q1;
  }
  if (kv) {
    const int tt = 2 * tp + fp;
    const float* l2 = lds + 16 * 4 * 64 * 4;
    const float* csk = (const float*)(ws + WS_CSQKV) + 512;
    if (w == fp) {
      float k[8];
#pragma unroll
      for (int g = 0; g < 2; ++g) {
        f32x4 s = {0.f, 0.f, 0.f, 0.f};
#pragma unroll
        for (int sw = 0; sw < 4; ++sw) s += *(const f32x4*)(l2 + ((sw * 4 + g) * 64 + lane) * 4);
        const f32x4 c = *(const f32x4*)(csk + 8 * g + 4 * h);
#pragma unroll
        for (int e = 0; e < 4; ++e) k[4 * g + e] = __builtin_fmaf(-ms[0], c[e], s[e]) * ms[1];
      }
      u32x4 ko = {pk2(k[0], k[1]), pk2(k[2], k[3]), pk2(k[4], k[5]), pk2(k[6], k[7])};
      *(u32x4*)(ws + WS_KV + (size_t)tt * 1024 + lane * 16) = ko;
    } else if (w == 2 + fp) {
      const float c = csk[r];
      char* vb = ws + WS_KV + KIMG_BYTES + (size_t)tt * 2 * VBLK;
#pragma unroll
      for (int sp = 0; sp < 2; ++sp) {
        float v[8];
#pragma unroll
        for (int g2 = 0; g2 < 2; ++g2) {
          const int g = 2 * sp + g2;
          f32x4 s = {0.f, 0.f, 0.f, 0.f};
#pragma unroll
          for (int sw = 0; sw < 4; ++sw) s += *(const f32x4*)(l2 + (((4 + sw) * 4 + g) * 64 + lane) * 4);
          const f32x4 mu = *(const f32x4*)(lms + 8 * g + 4 * h), rs = *(const f32x4*)(lms + 32 + 8 * g + 4 * h);
#pragma unroll
          for (int e = 0; e < 4; ++e) v[4 * g2 + e] = __builtin_fmaf(-mu[e], c, s[e]) * rs[e];
        }
        u32x4 vo = {pk2(v[0], v[1]), pk2(v[2], v[3]), pk2(v[4], v[5]), pk2(v[6], v[7])};
        if (r >= 16) *(u32x4*)(vb + sp * VBLK + h * 256 + (r - 16) * 16) = vo;
        else if (r == 0) { u32x4 ones = {0x3f803f80u, 0x3f803f80u, 0x3f803f80u, 0x3f803f80u}; *(u32x4*)(vb + sp * VBLK + 512 + h * 16) = ones; }
      }
    }
  }
}

DI void glds16(const void* g, unsigned lds_base) {
  unsigned sv;
  asm volatile("s_mov_b32 %0, m0\n\ts_mov_b32 m0, %2\n\ts_nop 0\n\tglobal_load_lds_dwordx4 %1, off\n\ts_mov_b32 m0, %0"
               : "=&s"(sv) : "v"(g), "s"(lds_base) : "memory");
}
typedef __attribute__((address_space(3))) const u32x4* lds_u4p;
constexpr int ATT_LDS = KIMG_BYTES + VIMG_BYTES + 8 * 1024;

DI void attn_tiles(const char __attribute__((address_space(3)))* kp, const char __attribute__((address_space(3)))* vp,
                   const u32x4 qf, const f32x16 negm, f32x16& o, int t0, int t1) {
  for (int t = t0; t < t1; ++t) {
    const u32x4 kf = *(lds_u4p)(kp + t * 1024);
    const u32x4 v0 = *(lds_u4p)(vp + t * 2 * VBLK);
    const u32x4 v1 = *(lds_u4p)(vp + t * 2 * VBLK + VBLK);
    f32x16 s = MFMA(kf, qf, negm);
#pragma unroll
    for (int i = 0; i < 16; ++i) s[i] = __builtin_amdgcn_exp2f(s[i]);
    u32x4 p0 = {pk2(s[0], s[1]), pk2(s[2], s[3]), pk2(s[4], s[5]), pk2(s[6], s[7])};
    u32x4 p1 = {pk2(s[8], s[9]), pk2(s[10], s[11]), pk2(s[12], s[13]), pk2(s[14], s[15])};
    o = MFMA(v0, p0, o);
    o = MFMA(v1, p1, o);
  }
}

__global__ void __launch_bounds__(512) attn_kernel(char* ws) {
  extern __shared__ __attribute__((aligned(16))) char smem[];
  const int lane = threadIdx.x & 63, r = lane & 31, h = lane >> 5;
  const int w = __builtin_amdgcn_readfirstlane(threadIdx.x >> 6);
  const int xcd = blockIdx.x & 7, idx = blockIdx.x >> 3;
  const int tt = 8 * xcd + (idx >> 2), head = 8 * (idx & 3) + w;
  const unsigned lds0 = (unsigned)(uintptr_t)smem;
  const char* kvg = ws + WS_KV + lane * 16;
  glds16(ws + WS_Q + ((size_t)tt * 32 + head) * 1024 + lane * 16, lds0 + KIMG_BYTES + VIMG_BYTES + w * 1024);
  glds16(kvg + w * 1024, lds0 + w * 1024);
  glds16(kvg + KIMG_BYTES + w * 1024, lds0 + KIMG_BYTES + w * 1024);
  if (w == 0) glds16(kvg + KIMG_BYTES + 8 * 1024, lds0 + KIMG_BYTES + 8 * 1024);
  asm volatile("s_waitcnt vmcnt(0)" ::: "memory");
  for (int p = 8 + w; p < 64; p += 8) glds16(kvg + p * 1024, lds0 + p * 1024);
  for (int p = 9 + w; p < 68; p += 8) glds16(kvg + KIMG_BYTES + p * 1024, lds0 + KIMG_BYTES + p * 1024);
  __builtin_amdgcn_s_barrier();
  typedef const char __attribute__((address_space(3)))* lcp;
  const lcp kp = (lcp)smem + lane * 16;
  const lcp vp = (lcp)smem + KIMG_BYTES + (r < 16 ? h * 256 + r * 16 : 512);
  const u32x4 qf = *(lds_u4p)((lcp)smem + KIMG_BYTES + VIMG_BYTES + w * 1024 + lane * 16);
  float m;
  {
    const u32x4 kf = *(lds_u4p)kp;
    f32x16 z = {};
    f32x16 s = MFMA(kf, qf, z);
    m = s[0];
#pragma unroll
    for (int i = 1; i < 16; ++i) m = __builtin_fmaxf(m, s[i]);
    m = xmax(m);
  }
  f32x16 negm, o = {};
#pragma unroll
  for (int i = 0; i < 16; ++i) negm[i] = -m;
  attn_tiles(kp, vp, qf, negm, o, 0, 8);
  asm volatile("s_waitcnt vmcnt(0)" ::: "memory");
  __builtin_amdgcn_s_barrier();
  attn_tiles(kp, vp, qf, negm, o, 8, 64);
  bool bad = !(o[8] < 1.0e18f);
#pragma unroll
  for (int i = 0; i < 8; ++i) bad |= !(__builtin_fabsf(o[i]) < 3.0e38f);
  if (__builtin_expect(__any(bad), 0)) {
    float tm = -INFINITY;
    for (int t = 0; t < 64; ++t) {
      const u32x4 kf = *(lds_u4p)(kp + t * 1024);
      f32x16 z = {};
      f32x16 s = MFMA(kf, qf, z);
#pragma unroll
      for (int i = 0; i < 16; ++i) tm = __builtin_fmaxf(tm, s[i]);
    }
    tm = xmax(tm);
#pragma unroll
    for (int i = 0; i < 16; ++i) { negm[i] = -tm; o[i] = 0.f; }
    attn_tiles(kp, vp, qf, negm, o, 0, 64);
  }
  const float rl = 1.0f / o[8];
  u32x4 oo = {pk2(o[0] * rl, o[1] * rl), pk2(o[2] * rl, o[3] * rl), pk2(o[4] * rl, o[5] * rl), pk2(o[6] * rl, o[7] * rl)};
  *(u32x4*)(ws + WS_O + ((size_t)tt * 32 + head) * 1024 + lane * 16) = oo;
}

template <int KS, int DEPTH, bool FINAL>
__global__ void __launch_bounds__(256) proj_res_kernel(const char* wt_g, const char* at_g, const char* res_g, char* y_g, char* st_g) {
  __shared__ __attribute__((aligned(16))) float lds[16 * 4 * 64 * 4];
  const int lane = threadIdx.x & 63, r = lane & 31, h = lane >> 5;
  const int w = __builtin_amdgcn_readfirstlane(threadIdx.x >> 6);
  const int xcd = blockIdx.x & 7, idx = blockIdx.x >> 3;
  const int tp = 4 * xcd + (idx >> 3), fp = idx & 7;
  const int fi = w >> 1, ti = w & 1;
  const int tt = 2 * tp + ti, ft = 2 * fp + fi;
  const char* rp = res_g + ((size_t)tt * KS_D + 2 * ft) * 1024 + lane * 16;
  const u32x4 r0 = *(const u32x4*)rp, r1 = *(const u32x4*)(rp + 1024);
  f32x16 acc[2][2] = {};
  const char* wt = wt_g + ((size_t)(2 * fp) * KS + (KS / 4) * w) * 1024 + lane * 16;
  const char* at = at_g + ((size_t)(2 * tp) * KS + (KS / 4) * w) * 1024 + lane * 16;
  gemm64_core<KS, DEPTH>(wt, at, acc, lane);
  f32x16 o;
  reduce4(acc, lds, w, lane, o);
  u32x4 y0, y1;
#pragma unroll
  for (int i = 0; i < 4; ++i) {
    y0[i] = pk2(o[2 * i] + bflo(r0[i]), o[2 * i + 1] + bfhi(r0[i]));
    y1[i] = pk2(o[8 + 2 * i] + bflo(r1[i]), o[8 + 2 * i + 1] + bfhi(r1[i]));
  }
  if (FINAL) {
    unsigned short* yp = (unsigned short*)y_g + (size_t)(32 * tt + r) * D_ + 32 * ft + 4 * h;
    u32x2 a0 = {y0[0], y0[1]}, a1 = {y0[2], y0[3]}, a2 = {y1[0], y1[1]}, a3 = {y1[2], y1[3]};
    *(u32x2*)(yp) = a0; *(u32x2*)(yp + 8) = a1; *(u32x2*)(yp + 16) = a2; *(u32x2*)(yp + 24) = a3;
  } else {
    char* yp = y_g + ((size_t)tt * KS_D + 2 * ft) * 1024 + lane * 16;
    *(u32x4*)yp = y0; *(u32x4*)(yp + 1024) = y1;
    float v[16];
#pragma unroll
    for (int i = 0; i < 4; ++i) { v[2 * i] = bflo(y0[i]); v[2 * i + 1] = bfhi(y0[i]); v[8 + 2 * i] = bflo(y1[i]); v[8 + 2 * i + 1] = bfhi(y1[i]); }
    const f32x2 st = tile_stats(v);
    if (h == 0) *(f32x2*)(st_g + ((size_t)(tt * 16 + ft) * 32 + r) * 8) = st;
  }
}

__global__ void __launch_bounds__(448) ffn1_kernel(const char* xt_g, const char* st_g, char* ws) {
  __shared__ __attribute__((aligned(16))) char smem[64 * 1024];
  const int lane = threadIdx.x & 63, r = lane & 31, h = lane >> 5;
  const int w = __builtin_amdgcn_readfirstlane(threadIdx.x >> 6);
  const int xcd = blockIdx.x & 7, idx = blockIdx.x >> 3;
  const int tp = 4 * xcd + (idx >> 3), fg = idx & 7;
  const int ft = 7 * fg + w;
  const unsigned lds0 = (unsigned)(uintptr_t)smem;
  const char* xg = xt_g + (size_t)(2 * tp) * KS_D * 1024 + lane * 16;
  for (int p = w; p < 64; p += 7) glds16(xg + p * 1024, lds0 + p * 1024);
  const char* wt = ws + WS_W1 + (size_t)ft * KS_D * 1024 + lane * 16;
  u32x4 wf[32];
#pragma unroll
  for (int i = 0; i < 32; ++i) wf[i] = *(const u32x4*)(wt + i * 1024);
  f32x2 st[2][8];
#pragma unroll
  for (int ti = 0; ti < 2; ++ti) {
    const char* sp = st_g + ((size_t)((2 * tp + ti) * 16 + 8 * h) * 32 + r) * 8;
#pragma unroll
    for (int i = 0; i < 8; ++i) st[ti][i] = *(const f32x2*)(sp + i * 256);
  }
  f32x4 cs[4];
  {
    const float* cp = (const float*)(ws + WS_CS1) + 32 * ft + 4 * h;
#pragma unroll
    for (int g = 0; g < 4; ++g) cs[g] = *(const f32x4*)(cp + 8 * g);
  }
  asm volatile("s_waitcnt vmcnt(52)\n\ts_barrier" ::: "memory");
  typedef const char __attribute__((address_space(3)))* lcp;
  const lcp xp = (lcp)smem + lane * 16;
  f32x16 acc0 = {}, acc1 = {};
#pragma unroll
  for (int i = 0; i < 32; ++i) {
    const u32x4 x0 = *(lds_u4p)(xp + i * 1024), x1 = *(lds_u4p)(xp + 32 * 1024 + i * 1024);
    acc0 = MFMA(wf[i], x0, acc0);
    acc1 = MFMA(wf[i], x1, acc1);
  }
#pragma unroll
  for (int ti = 0; ti < 2; ++ti) {
    const f32x2 ms = combine_stats(st[ti]);
    const f32x16 o = ti ? acc1 : acc0;
    const float nm = -ms[0], sc = ms[1];
    u32x4 q0, q1;
#pragma unroll
    for (int g = 0; g < 4; ++g) {
      const float a = __builtin_fmaxf(__builtin_fmaf(nm, cs[g][0], o[4 * g]) * sc, 0.f), b = __builtin_fmaxf(__builtin_fmaf(nm, cs[g][1], o[4 * g + 1]) * sc, 0.f);
      const float c = __builtin_fmaxf(__builtin_fmaf(nm, cs[g][2], o[4 * g + 2]) * sc, 0.f), d = __builtin_fmaxf(__builtin_fmaf(nm, cs[g][3], o[4 * g + 3]) * sc, 0.f);
      if (g < 2) { q0[2 * g] = pk2(a, b); q0[2 * g + 1] = pk2(c, d); }
      else { q1[2 * (g - 2)] = pk2(a, b); q1[2 * (g - 2) + 1] = pk2(c, d); }
    }
    char* hp = ws + WS_H + ((size_t)(2 * tp + ti) * KS_F + 2 * ft) * 1024 + lane * 16;
    *(u32x4*)hp = q0; *(u32x4*)(hp + 1024) = q1;
  }
}

extern "C" void kernel_launch(void* const* d_in, const int* in_sizes, int n_in, void* d_out, int out_size, void* d_ws, size_t ws_size,
                              hipStream_t stream) {
  const unsigned short* x = (const unsigned short*)d_in[0];
  const unsigned short* wq = (const unsigned short*)d_in[1];
  const unsigned short* wk = (const unsigned short*)d_in[2];
  const unsigned short* wv = (const unsigned short*)d_in[3];
  const unsigned short* wo = (const unsigned short*)d_in[4];
  const unsigned short* w1 = (const unsigned short*)d_in[6];
  const unsigned short* w2 = (const unsigned short*)d_in[7];
  char* ws = (char*)d_ws;
  (void)hipFuncSetAttribute((const void*)attn_kernel, hipFuncAttributeMaxDynamicSharedMemorySize, ATT_LDS);
  prep_kernel<<<1024, 256, 0, stream>>>(x, wq, wk, wv, wo, w1, w2, ws);
  for (int layer = 0; layer < 2; ++layer) {
    qkv_kernel<<<256, 256, 0, stream>>>(ws + WS_XA, ws + WS_STA, ws);
    attn_kernel<<<256, 512, ATT_LDS, stream>>>(ws);
    proj_res_kernel<KS_D, 8, false><<<256, 256, 0, stream>>>(ws + WS_WO, ws + WS_O, ws + WS_XA, ws + WS_XB, ws + WS_STB);
    ffn1_kernel<<<256, 448, 0, stream>>>(ws + WS_XB, ws + WS_STB, ws);
    if (layer == 0)
      proj_res_kernel<KS_F, 7, false><<<256, 256, 0, stream>>>(ws + WS_W2, ws + WS_H, ws + WS_XB, ws + WS_XA, ws + WS_STA);
    else
      proj_res_kernel<KS_F, 7, true><<<256, 256, 0, stream>>>(ws + WS_W2, ws + WS_H, ws + WS_XB, (char*)d_out, ws + WS_STA);
  }
}
```

```cpp
#include <hip/hip_runtime.h>
#include <hip/hip_bf16.h>
#include <stdint.h>
#include <type_traits>

typedef __attribute__((ext_vector_type(8))) __bf16 bf16x8;
typedef __attribute__((ext_vector_type(2))) __bf16 bf16x2;
typedef __attribute__((ext_vector_type(16))) float f32x16;
typedef __attribute__((ext_vector_type(4))) float f32x4;
typedef __attribute__((ext_vector_type(2))) float f32x2;
typedef __attribute__((ext_vector_type(4))) unsigned u32x4;
typedef __attribute__((ext_vector_type(2))) unsigned u32x2;
#define DI __device__ __forceinline__
#define MFMA(a, b, c) __builtin_amdgcn_mfma_f32_32x32x16_bf16(__builtin_bit_cast(bf16x8, (a)), __builtin_bit_cast(bf16x8, (b)), (c), 0, 0, 0)
#define SBAR() __builtin_amdgcn_sched_barrier(0)
#define PIN(x) asm volatile("" : "+v"(x))
#define PINA(x) asm volatile("" : "+a"(x))
#define LAS __attribute__((address_space(3)))
#define GAS __attribute__((address_space(1)))

constexpr int S_ = 2048, D_ = 512, DFF = 1792;
constexpr int NTT = S_ / 32;
constexpr int KS_D = D_ / 16;
constexpr int KS_F = DFF / 16;
constexpr int VBLK = 544;
constexpr int KIMG_BYTES = NTT * 1024;
constexpr int VIMG_BYTES = NTT * 2 * VBLK;
constexpr float QSCALE = 0.25f * 1.4426950408889634f;

constexpr int MB2 = 2 * 1024 * 1024;
constexpr int WS_WQKV = 0;
constexpr int WS_WO   = WS_WQKV + 17 * 32 * 1024;
constexpr int WS_W1   = WS_WO + 16 * 32 * 1024;
constexpr int WS_W2   = WS_W1 + 56 * 32 * 1024;
constexpr int WS_CSQKV = WS_W2 + 16 * 112 * 1024;
constexpr int WS_CS1  = WS_CSQKV + 4096;
constexpr int WS_X0   = WS_CS1 + 8192;
constexpr int WS_ST0  = WS_X0 + MB2;
constexpr int WS_Q    = WS_ST0 + 256 * 1024;
constexpr int WS_KV   = WS_Q + MB2;
constexpr int KV_STR  = 128 * 1024;
constexpr int HB_O = 0, HB_X1 = MB2, HB_ST1 = 2 * MB2, HB_H = HB_ST1 + 256 * 1024, HB_STR = HB_H + S_ * DFF * 2;
constexpr int WS_HB   = WS_KV + 2 * KV_STR;
constexpr int WS_X2   = WS_HB + 2 * HB_STR;
constexpr int WS_ST2  = WS_X2 + MB2;
constexpr int WS_END  = WS_ST2 + 256 * 1024;

#define STAMP(k, i)

DI unsigned pk2(float a, float b) { f32x2 v = {a, b}; bf16x2 r = __builtin_convertvector(v, bf16x2); return __builtin_bit_cast(unsigned, r); }
DI float bflo(unsigned u) { return __uint_as_float(u << 16); }
DI float bfhi(unsigned u) { return __uint_as_float(u & 0xffff0000u); }
DI float xsum(float v) {
  auto rr = __builtin_amdgcn_permlane32_swap(__float_as_uint(v), __float_as_uint(v), false, false);
  return __uint_as_float(rr[0]) + __uint_as_float(rr[1]);
}
DI float xmax(float v) {
  auto rr = __builtin_amdgcn_permlane32_swap(__float_as_uint(v), __float_as_uint(v), false, false);
  return __builtin_fmaxf(__uint_as_float(rr[0]), __uint_as_float(rr[1]));
}
DI float lowhalf(float v) {
  auto rr = __builtin_amdgcn_permlane32_swap(__float_as_uint(v), __float_as_uint(v), false, false);
  return __uint_as_float(rr[0]);
}
DI int perm16(int h, int j) { return 8 * (j >> 2) + 4 * h + (j & 3); }

DI f32x2 tile_stats(const float* v) {
  float s = 0.f;
#pragma unroll
  for (int i = 0; i < 16; ++i) s += v[i];
  s = xsum(s);
  const float mu = s * (1.0f / 32.0f);
  float m2 = 0.f;
#pragma unroll
  for (int i = 0; i < 16; ++i) { const float d = v[i] - mu; m2 = __builtin_fmaf(d, d, m2); }
  m2 = xsum(m2);
  f32x2 r = {s, m2};
  return r;
}
DI f32x2 combine_stats(const f32x2* p) {
  float s = 0.f;
#pragma unroll
  for (int i = 0; i < 8; ++i) s += p[i][0];
  s = xsum(s);
  const float mean = s * (1.0f / 512.0f);
  float m2 = 0.f;
#pragma unroll
  for (int i = 0; i < 8; ++i) { const float d = p[i][0] * (1.0f / 32.0f) - mean; m2 += p[i][1] + 32.0f * d * d; }
  m2 = xsum(m2);
  const float var = m2 * (1.0f / 512.0f);
  f32x2 r = {mean, 1.0f / __builtin_sqrtf(var + 1e-5f)};
  return r;
}

typedef __amdgpu_buffer_rsrc_t rsrc_t;
constexpr int AUX_SC1 = 16;
DI rsrc_t ws_rsrc(char* ws) { return __builtin_amdgcn_make_buffer_rsrc((void*)ws, 0, WS_END, 0x00020000); }
DI u32x4 ld16(rsrc_t rs, int voff, int soff) { return __builtin_amdgcn_raw_buffer_load_b128(rs, voff, soff, 0); }
DI u32x4 ld16c(rsrc_t rs, int voff, int soff) { return __builtin_amdgcn_raw_buffer_load_b128(rs, voff, soff, AUX_SC1); }
DI u32x2 ld8c(rsrc_t rs, int voff, int soff) { return __builtin_amdgcn_raw_buffer_load_b64(rs, voff, soff, AUX_SC1); }
DI void st16(rsrc_t rs, u32x4 v, int voff, int soff) { __builtin_amdgcn_raw_buffer_store_b128(v, rs, voff, soff, 0); }
DI int sop(int x) { asm volatile("" : "+s"(x)); return x; }
#define LDK(base, k) ld16(rs, lo + ((k) & 3) * 1024, (base) + ((k) >> 2) * 4096)
#define LDKC(base, k) ld16c(rs, lo + ((k) & 3) * 1024, (base) + ((k) >> 2) * 4096)
constexpr unsigned SENT = 0xffffffffu;
DI unsigned clean(unsigned u) { return u < SENT ? u : SENT - 1u; }
DI void st16c(rsrc_t rs, u32x4 v, int voff, int soff) {
  u32x4 c = {clean(v[0]), clean(v[1]), clean(v[2]), clean(v[3])};
  __builtin_amdgcn_raw_buffer_store_b128(c, rs, voff, soff, AUX_SC1);
}
DI void st8c(rsrc_t rs, f32x2 v, int voff, int soff) {
  u32x2 c = {clean(__float_as_uint(v[0])), clean(__float_as_uint(v[1]))};
  __builtin_amdgcn_raw_buffer_store_b64(c, rs, voff, soff, AUX_SC1);
}
DI unsigned umax3(unsigned a, unsigned b, unsigned c) { unsigned m = a > b ? a : b; return m > c ? m : c; }
DI unsigned fold4(unsigned m, u32x4 v) { return umax3(umax3(m, v[0], v[1]), v[2], v[3]); }
constexpr int SPIN_CAP = 1 << 15;

#define HANDOFF_LOAD(N, DST, LOADEXPR, FOLD) do { \
    int spins_ = 0; \
    for (;;) { \
      asm volatile("" ::: "memory"); \
      _Pragma("unroll") for (int i = 0; i < (N); ++i) DST[i] = LOADEXPR; \
      unsigned m_ = 0; \
      _Pragma("unroll") for (int i = 0; i < (N); ++i) m_ = FOLD; \
      if (!__any(m_ == SENT) || ++spins_ > SPIN_CAP) break; \
    } } while (0)

DI void tile_weight(const unsigned short* W, int ldw, int col0, int s, char* dst, int lane) {
  const int r = lane & 31, h = lane >> 5;
  unsigned short e[8];
#pragma unroll
  for (int j = 0; j < 8; ++j) e[j] = W[(size_t)(16 * s + perm16(h, j)) * ldw + col0 + r];
  u32x4 o;
#pragma unroll
  for (int j = 0; j < 4; ++j) o[j] = (unsigned)e[2 * j] | ((unsigned)e[2 * j + 1] << 16);
  *(u32x4*)(dst + lane * 16) = o;
}

__global__ void __launch_bounds__(256) prep_kernel(const unsigned short* x, const unsigned short* wq, const unsigned short* wk,
                                                   const unsigned short* wv, const unsigned short* wo, const unsigned short* w1,
                                                   const unsigned short* w2, char* ws) {
  const int lane = threadIdx.x & 63, r = lane & 31, h = lane >> 5;
  const int gw = blockIdx.x * 4 + (threadIdx.x >> 6);
  const int nw = gridDim.x * 4;
  {
    const u32x4 sv = {SENT, SENT, SENT, SENT};
    for (int p = gw; p < 2048 + 128; p += nw)
      *(u32x4*)(ws + (p < 2048 ? WS_HB + HB_O + p * 1024 : WS_KV + KV_STR + (p - 2048) * 1024) + lane * 16) = sv;
  }
  for (int job = gw; job < 5664 + 73; job += nw) {
    if (job < 512) { const int ft = job >> 5, s = job & 31; tile_weight(wq, 512, 32 * ft, s, ws + WS_WQKV + (size_t)job * 1024, lane); }
    else if (job < 544) {
      const int s = job - 512;
      unsigned short e[8];
      const unsigned short* W = r < 16 ? wk : wv;
#pragma unroll
      for (int j = 0; j < 8; ++j) e[j] = W[(16 * s + perm16(h, j)) * 16 + (r & 15)];
      u32x4 o;
#pragma unroll
      for (int j = 0; j < 4; ++j) o[j] = (unsigned)e[2 * j] | ((unsigned)e[2 * j + 1] << 16);
      *(u32x4*)(ws + WS_WQKV + (size_t)job * 1024 + lane * 16) = o;
    }
    else if (job < 1056) { const int jj = job - 544, ft = jj >> 5, s = jj & 31; tile_weight(wo, 512, 32 * ft, s, ws + WS_WO + (size_t)jj * 1024, lane); }
    else if (job < 2848) { const int jj = job - 1056, ft = jj >> 5, s = jj & 31; tile_weight(w1, DFF, 32 * ft, s, ws + WS_W1 + (size_t)jj * 1024, lane); }
    else if (job < 4640) { const int jj = job - 2848, ft = jj / 112, s = jj % 112; tile_weight(w2, 512, 32 * ft, s, ws + WS_W2 + (size_t)jj * 1024, lane); }
    else if (job < 5664) {
      const int jj = job - 4640, tt = jj >> 4, ft = jj & 15;
      const unsigned short* row = x + (size_t)(32 * tt + r) * 512 + 32 * ft + 4 * h;
      u32x2 a[4];
#pragma unroll
      for (int i = 0; i < 4; ++i) a[i] = *(const u32x2*)(row + 8 * i);
      float v[16];
#pragma unroll
      for (int i = 0; i < 4; ++i) { v[4 * i] = bflo(a[i][0]); v[4 * i + 1] = bfhi(a[i][0]); v[4 * i + 2] = bflo(a[i][1]); v[4 * i + 3] = bfhi(a[i][1]); }
      const f32x2 st = tile_stats(v);
      char* xt = ws + WS_X0 + ((size_t)tt * KS_D + 2 * ft) * 1024 + lane * 16;
      u32x4 o0 = {a[0][0], a[0][1], a[1][0], a[1][1]}, o1 = {a[2][0], a[2][1], a[3][0], a[3][1]};
      *(u32x4*)xt = o0; *(u32x4*)(xt + 1024) = o1;
      if (h == 0) *(f32x2*)(ws + WS_ST0 + ((size_t)(tt * 16 + ft) * 32 + r) * 8) = st;
    }
    else {
      const int jj = job - 5664;
      const unsigned short* W; int ldw, col; float* dst;
      if (jj < 16) { W = wq; ldw = 512; col = 32 * jj + r; dst = (float*)(ws + WS_CSQKV) + 32 * jj + r; }
      else if (jj == 16) { W = r < 16 ? wk : wv; ldw = 16; col = r & 15; dst = (float*)(ws + WS_CSQKV) + 512 + r; }
      else { W = w1; ldw = DFF; col = 32 * (jj - 17) + r; dst = (float*)(ws + WS_CS1) + 32 * (jj - 17) + r; }
      float s = 0.f;
#pragma unroll 16
      for (int k = 0; k < 256; ++k) s += __uint_as_float((unsigned)W[(size_t)(256 * h + k) * ldw + col] << 16);
      s = xsum(s);
      if (h == 0) *dst = s;
    }
  }
}

DI void red_put(const f32x16& a, LAS float* lds, int w, int qp, int lane) {
  LAS float* p = lds + ((((w ^ qp) * 3 + (qp - 1)) * 4) * 64 + lane) * 4;
#pragma unroll
  for (int g = 0; g < 4; ++g) {
    f32x4 v = {a[4 * g], a[4 * g + 1], a[4 * g + 2], a[4 * g + 3]};
    *(LAS f32x4*)(p + g * 256) = v;
  }
}
DI void red_get(f32x16& o, const LAS float* lds, int w, int lane) {
  const LAS float* p = lds + ((w * 3 * 4) * 64 + lane) * 4;
#pragma unroll
  for (int sl = 0; sl < 3; ++sl) {
#pragma unroll
    for (int g = 0; g < 4; ++g) {
      const f32x4 v = *(const LAS f32x4*)(p + (sl * 4 + g) * 256);
      o[4 * g] += v[0]; o[4 * g + 1] += v[1]; o[4 * g + 2] += v[2]; o[4 * g + 3] += v[3];
    }
  }
}
DI f32x16 unpack16(const u32x4 r0, const u32x4 r1) {
  f32x16 o;
#pragma unroll
  for (int i = 0; i < 4; ++i) { o[2 * i] = bflo(r0[i]); o[2 * i + 1] = bfhi(r0[i]); o[8 + 2 * i] = bflo(r1[i]); o[8 + 2 * i + 1] = bfhi(r1[i]); }
  return o;
}
constexpr int RED_BYTES = 12 * 4096;
constexpr int KVRED_BYTES = 8 * 4096;
#define BAR() do { asm volatile("s_waitcnt lgkmcnt(0)" ::: "memory"); __builtin_amdgcn_s_barrier(); asm volatile("" ::: "memory"); } while (0)

template <bool KV>
DI void qkv_finish(rsrc_t rs, LAS char* L, const u32x4 (&wf)[2][8], const u32x4 (&xf)[2][8], const u32x4 (&wkv)[8], f32x2 ms,
                   const f32x4 (&cs)[4], const f32x4 (&csk)[2], float csv, int tp, int fp, int w, int lane, int kvbase,
                   u32x4& q0, u32x4& q1) {
  const int r = lane & 31, h = lane >> 5, ti = w & 1;
  LAS float* red = (LAS float*)L;
  LAS float* l2 = (LAS float*)(L + RED_BYTES);
  LAS float* lms = (LAS float*)(L + RED_BYTES + KVRED_BYTES);
  if (KV) {
    f32x16 accK = {}, accV = {};
    const bool sel = (fp ^ ti) != 0;
#pragma unroll
    for (int i = 0; i < 8; ++i) {
      const u32x4 b = sel ? xf[1][i] : xf[0][i];
      accV = MFMA(b, wkv[i], accV);
      accK = MFMA(wkv[i], b, accK);
    }
#pragma unroll
    for (int g = 0; g < 4; ++g) {
      f32x4 u = {accV[4 * g], accV[4 * g + 1], accV[4 * g + 2], accV[4 * g + 3]};
      *(LAS f32x4*)(l2 + (((4 + w) * 4 + g) * 64 + lane) * 4) = u;
      if (g < 2) { f32x4 v = {accK[4 * g], accK[4 * g + 1], accK[4 * g + 2], accK[4 * g + 3]}; *(LAS f32x4*)(l2 + ((w * 4 + g) * 64 + lane) * 4) = v; }
    }
    if (w == fp && h == 0) { lms[r] = ms[0]; lms[32 + r] = ms[1]; }
    BAR();
    const int tt = 2 * tp + fp;
    if (w == fp) {
      float k[8];
#pragma unroll
      for (int g = 0; g < 2; ++g) {
        f32x4 s = {0.f, 0.f, 0.f, 0.f};
#pragma unroll
        for (int sw = 0; sw < 4; ++sw) s += *(const LAS f32x4*)(l2 + ((sw * 4 + g) * 64 + lane) * 4);
#pragma unroll
        for (int e = 0; e < 4; ++e) k[4 * g + e] = __builtin_fmaf(-ms[0], csk[g][e], s[e]) * ms[1];
      }
      u32x4 ko = {pk2(k[0], k[1]), pk2(k[2], k[3]), pk2(k[4], k[5]), pk2(k[6], k[7])};
      st16c(rs, ko, lane * 16, kvbase + tt * 2048);
    } else if (w >= 2) {
      const int sp = w - 2;
      float v[8];
#pragma unroll
      for (int g2 = 0; g2 < 2; ++g2) {
        const int g = 2 * sp + g2;
        f32x4 s = {0.f, 0.f, 0.f, 0.f};
#pragma unroll
        for (int sw = 0; sw < 4; ++sw) s += *(const LAS f32x4*)(l2 + (((4 + sw) * 4 + g) * 64 + lane) * 4);
        const f32x4 mu = *(const LAS f32x4*)(lms + 8 * g + 4 * h), rsd = *(const LAS f32x4*)(lms + 32 + 8 * g + 4 * h);
#pragma unroll
        for (int e = 0; e < 4; ++e) v[4 * g2 + e] = __builtin_fmaf(-mu[e], csv, s[e]) * rsd[e];
      }
      u32x4 vo = {pk2(v[0], v[1]), pk2(v[2], v[3]), pk2(v[4], v[5]), pk2(v[6], v[7])};
      if (r >= 16) st16c(rs, vo, sp * 512 + h * 256 + (r - 16) * 16, kvbase + tt * 2048 + 1024);
    }
  }
#pragma unroll
  for (int qp = 1; qp < 4; ++qp) {
    f32x16 a = {};
#pragma unroll
    for (int i = 0; i < 8; ++i) a = MFMA(wf[qp >> 1][i], xf[qp & 1][i], a);
    red_put(a, red, w, qp, lane);
  }
  f32x16 o = {};
#pragma unroll
  for (int i = 0; i < 8; ++i) o = MFMA(wf[0][i], xf[0][i], o);
  BAR();
  red_get(o, red, w, lane);
  const float nm = -ms[0], sc = ms[1] * QSCALE;
#pragma unroll
  for (int g = 0; g < 4; ++g) {
    const float a = __builtin_fmaf(nm, cs[g][0], o[4 * g]) * sc, b = __builtin_fmaf(nm, cs[g][1], o[4 * g + 1]) * sc;
    const float c = __builtin_fmaf(nm, cs[g][2], o[4 * g + 2]) * sc, d = __builtin_fmaf(nm, cs[g][3], o[4 * g + 3]) * sc;
    if (g < 2) { q0[2 * g] = pk2(a, b); q0[2 * g + 1] = pk2(c, d); }
    else { q1[2 * (g - 2)] = pk2(a, b); q1[2 * (g - 2) + 1] = pk2(c, d); }
  }
}

constexpr int QKV_LDS = RED_BYTES + KVRED_BYTES + 256;
__global__ void __launch_bounds__(256, 1) qkv_kernel(char* ws) {
  extern __shared__ __attribute__((aligned(16))) char smem[];
  LAS char* L = (LAS char*)smem;
  const rsrc_t rs = ws_rsrc(ws);
  const int lane = threadIdx.x & 63, r = lane & 31, h = lane >> 5;
  const int w = __builtin_amdgcn_readfirstlane(threadIdx.x >> 6);
  const int xcd = blockIdx.x & 7, idx = blockIdx.x >> 3;
  const int tp = 4 * xcd + (idx >> 3), fp = idx & 7;
  const int fi = w >> 1, ti = w & 1;
  const bool kv = fp < 2;
  u32x4 wf[2][8], xf[2][8], wkv[8];
  const int wa_ = WS_WQKV + ((2 * fp + fi) * KS_D + 8 * w) * 1024, wb_ = WS_WQKV + ((2 * fp + (fi ^ 1)) * KS_D + 8 * w) * 1024;
  const int xa_ = WS_X0 + ((2 * tp + ti) * KS_D + 8 * w) * 1024, xb_ = WS_X0 + ((2 * tp + (ti ^ 1)) * KS_D + 8 * w) * 1024;
#pragma unroll
  for (int i = 0; i < 8; ++i) {
    wf[0][i] = ld16(rs, lane * 16, wa_ + i * 1024); xf[1][i] = ld16(rs, lane * 16, xb_ + i * 1024);
    wf[1][i] = ld16(rs, lane * 16, wb_ + i * 1024); xf[0][i] = ld16(rs, lane * 16, xa_ + i * 1024);
  }
  if (kv) {
#pragma unroll
    for (int i = 0; i < 8; ++i) wkv[i] = ld16(rs, lane * 16, WS_WQKV + (16 * KS_D + 8 * w + i) * 1024);
  }
  f32x2 st[8];
#pragma unroll
  for (int i = 0; i < 8; ++i) st[i] = __builtin_bit_cast(f32x2, __builtin_amdgcn_raw_buffer_load_b64(rs, (8 * h * 32 + r) * 8 + i * 256, WS_ST0 + (2 * tp + ti) * 16 * 256, 0));
  f32x4 cs[4], csk[2];
#pragma unroll
  for (int g = 0; g < 4; ++g) cs[g] = __builtin_bit_cast(f32x4, ld16(rs, h * 16 + g * 32, WS_CSQKV + 128 * (2 * fp + fi)));
#pragma unroll
  for (int g = 0; g < 2; ++g) csk[g] = __builtin_bit_cast(f32x4, ld16(rs, h * 16 + g * 32, WS_CSQKV + 2048));
  const float csv = __builtin_bit_cast(float, __builtin_amdgcn_raw_buffer_load_b32(rs, r * 4, WS_CSQKV + 2048, 0));
  SBAR();
  const f32x2 ms = combine_stats(st);
  u32x4 q0, q1;
  if (kv) qkv_finish<true>(rs, L, wf, xf, wkv, ms, cs, csk, csv, tp, fp, w, lane, WS_KV, q0, q1);
  else qkv_finish<false>(rs, L, wf, xf, wkv, ms, cs, csk, csv, tp, fp, w, lane, WS_KV, q0, q1);
  const int qo = WS_Q + ((2 * tp + ti) * 32 + 2 * (2 * fp + fi)) * 1024;
  st16(rs, q0, lane * 16, qo); st16(rs, q1, lane * 16, qo + 1024);
}

typedef LAS const u32x4* lds_u4p;
typedef const LAS char* lcp;
DI lcp opaque(lcp p) { unsigned a = (unsigned)(uintptr_t)p; asm volatile("" : "+v"(a)); return (lcp)(uintptr_t)a; }
constexpr int CH_X = RED_BYTES + KVRED_BYTES;
constexpr int W2L_HI = CH_X + 65536;
constexpr int MAIN_LDS = W2L_HI + 16384;
static_assert(MAIN_LDS <= 160 * 1024, "LDS");
constexpr int RES_OFF = KIMG_BYTES + VIMG_BYTES + 2048;
static_assert(W2L_HI >= RES_OFF + 8192, "K/V image + residual must fit");
DI void glds16(const void* g, unsigned lds_base) {
  unsigned sv;
  asm volatile("s_mov_b32 %0, m0\n\ts_mov_b32 m0, %2\n\ts_nop 0\n\tglobal_load_lds_dwordx4 %1, off\n\ts_mov_b32 m0, %0"
               : "=&s"(sv) : "v"(g), "s"(lds_base) : "memory");
}
#define EX(x) __builtin_amdgcn_exp2f(x)
DI f32x16 mfma_v(const u32x4 a, const u32x4 b, const f32x16 c) {
  f32x16 d;
  asm volatile("v_mfma_f32_32x32x16_bf16 %0, %1, %2, %3" : "=&v"(d) : "v"(a), "v"(b), "v"(c));
  return d;
}

DI void attn_simple(lcp kp, lcp vp, const u32x4 qf, f32x16& o) {
  float tm = -INFINITY;
  for (int t = 0; t < 64; ++t) {
    const u32x4 kx = *(lds_u4p)(kp + t * 1024);
    f32x16 z = {};
    f32x16 s = MFMA(kx, qf, z);
#pragma unroll
    for (int i = 0; i < 16; ++i) tm = __builtin_fmaxf(tm, s[i]);
  }
  tm = xmax(tm);
  f32x16 negm;
#pragma unroll
  for (int i = 0; i < 16; ++i) { negm[i] = -tm; o[i] = 0.f; }
  for (int t = 0; t < 64; ++t) {
    const u32x4 kf = *(lds_u4p)(kp + t * 1024);
    const u32x4 v0 = *(lds_u4p)(vp + t * 2 * VBLK);
    const u32x4 v1 = *(lds_u4p)(vp + t * 2 * VBLK + VBLK);
    f32x16 s = MFMA(kf, qf, negm);
#pragma unroll
    for (int i = 0; i < 16; ++i) s[i] = EX(s[i]);
    u32x4 p0 = {pk2(s[0], s[1]), pk2(s[2], s[3]), pk2(s[4], s[5]), pk2(s[6], s[7])};
    u32x4 p1 = {pk2(s[8], s[9]), pk2(s[10], s[11]), pk2(s[12], s[13]), pk2(s[14], s[15])};
    o = MFMA(v0, p0, o);
    o = MFMA(v1, p1, o);
  }
}

DI void publish_x(rsrc_t rs, const f32x16& o, int xbase, int stbase, int tt, int ft, int lane, u32x4& y0, u32x4& y1) {
  const int r = lane & 31, h = lane >> 5;
#pragma unroll
  for (int i = 0; i < 4; ++i) { y0[i] = pk2(o[2 * i], o[2 * i + 1]); y1[i] = pk2(o[8 + 2 * i], o[8 + 2 * i + 1]); }
  const int yo = sop(xbase + (tt * KS_D + 2 * ft) * 1024);
  st16c(rs, y0, lane * 16, yo); st16c(rs, y1, lane * 16 + 1024, yo);
  SBAR();
  float v[16];
#pragma unroll
  for (int i = 0; i < 4; ++i) { v[2 * i] = bflo(y0[i]); v[2 * i + 1] = bfhi(y0[i]); v[8 + 2 * i] = bflo(y1[i]); v[8 + 2 * i + 1] = bfhi(y1[i]); }
  const f32x2 st = tile_stats(v);
  st8c(rs, st, h ? 0x7ffffff0 - WS_END : r * 8, stbase + (tt * 16 + ft) * 256);
}

constexpr int ord28(int pos) { return pos < 8 ? pos : pos < 16 ? pos + 6 : pos < 22 ? pos - 8 : pos; }

__global__ void __launch_bounds__(256, 1) main_kernel(char* ws, unsigned short* out) {
  extern __shared__ __attribute__((aligned(16))) char smem[];
  LAS char* L = (LAS char*)smem;
  LAS float* red = (LAS float*)L;
  const rsrc_t rs = ws_rsrc(ws);
  const unsigned lds0 = (unsigned)(uintptr_t)smem;
  const int w = __builtin_amdgcn_readfirstlane(threadIdx.x >> 6);
  const int xcd = blockIdx.x & 7, idx = blockIdx.x >> 3;
  const int tp = 4 * xcd + (idx >> 3), fp = idx & 7;
  const int fi = w >> 1, ti = w & 1;
  const int tt = 2 * tp + ti, ft = 2 * fp + fi;
  const int tto = 2 * tp + (ti ^ 1), fto = 2 * fp + (fi ^ 1);
  int lane = threadIdx.x & 63, r = lane & 31, h = lane >> 5, lo = lane * 16;
#define RELANE() do { lane = __builtin_amdgcn_mbcnt_hi(~0u, __builtin_amdgcn_mbcnt_lo(~0u, 0u)); asm volatile("" : "+v"(lane)); lane &= 63; r = lane & 31; h = lane >> 5; lo = lane * 16; } while (0)
  const int own = (tt * KS_D + 2 * ft) * 1024;
  const int fta = 7 * fp + w, ftb = 7 * fp + (w < 2 ? 4 + w : 6);
  const int w1ao = WS_W1 + fta * KS_D * 1024, w1bo = WS_W1 + ftb * KS_D * 1024;
  const int woa = WS_WO + (ft * KS_D + 8 * w) * 1024, wob = WS_WO + (fto * KS_D + 8 * w) * 1024;
  const int w2a = WS_W2 + (ft * KS_F + 28 * w) * 1024, w2b = WS_W2 + (fto * KS_F + 28 * w) * 1024;
  const int hoa = ((2 * tp) * KS_F + 2 * fta) * 1024, hob = ((2 * tp) * KS_F + 2 * ftb) * 1024;

  u32x4 q0 = ld16(rs, lo, WS_Q + own), q1 = ld16(rs, lo, WS_Q + own + 1024);
  {
    const u32x4 res0 = ld16(rs, lo, WS_X0 + own), res1 = ld16(rs, lo, WS_X0 + own + 1024);
    *(LAS u32x4*)(L + RES_OFF + w * 2048 + lo) = res0; *(LAS u32x4*)(L + RES_OFF + w * 2048 + 1024 + lo) = res1;
  }

#pragma unroll 1
  for (int layer = 0; layer < 2; ++layer) {
    const int hb = WS_HB + layer * HB_STR;
    const int KID = 2 + layer;
    STAMP(KID, 0);
    RELANE();
    u32x4 wof[2][8], w1a[32], w1b[32];
    f32x16 o0, o1;
    {
      const int kva_ = sop(WS_KV + layer * KV_STR + 8 * w * 1024), kvb_ = sop(WS_KV + layer * KV_STR + (32 + 24 * w) * 1024);
      u32x4 ka[8], kb[24];
#define KVA(i) LDKC(kva_, i)
#define KVB(i) LDKC(kvb_, i)
#pragma unroll
      for (int i = 0; i < 8; ++i) ka[i] = KVA(i);
#pragma unroll
      for (int i = 0; i < 24; ++i) kb[i] = KVB(i);
      SBAR();
      {
        const unsigned c = (lane & 1) ? 0u : 0x3f803f80u;
        const u32x4 cv = {c, c, c, c};
        *(LAS u32x4*)(L + KIMG_BYTES + (32 * w + (lane >> 1)) * VBLK + 512 + (lane & 1) * 16) = cv;
      }
      LAS char* kdst = L + lo;
      LAS char* vdst = L + KIMG_BYTES + h * VBLK + r * 16;
      {
        unsigned m0 = 0;
#pragma unroll
        for (int i = 0; i < 8; ++i) m0 = fold4(m0, ka[i]);
        if (__any(m0 == SENT)) { HANDOFF_LOAD(8, ka, KVA(i), fold4(m_, ka[i])); }
#pragma unroll
        for (int i = 0; i < 4; ++i) { *(LAS u32x4*)(kdst + (4 * w + i) * 1024) = ka[2 * i]; *(LAS u32x4*)(vdst + (4 * w + i) * 2 * VBLK) = ka[2 * i + 1]; }
      }
      BAR();
      STAMP(KID, 12);
      const lcp kp0 = (lcp)L + lo;
      const lcp vp0 = (lcp)L + KIMG_BYTES + (r < 16 ? h * 256 + r * 16 : (r > 16) ? 528 : 512);
      lcp kq = opaque(kp0), vq = opaque(vp0);
      f32x16 ng0, ng1;
      u32x4 kf = *(lds_u4p)kq;
      {
        f32x16 z = {};
        f32x16 s = MFMA(kf, q0, z), s2 = MFMA(kf, q1, z);
        float m = s[0], m2 = s2[0];
#pragma unroll
        for (int i = 1; i < 16; ++i) { m = __builtin_fmaxf(m, s[i]); m2 = __builtin_fmaxf(m2, s2[i]); }
        m = xmax(m); m2 = xmax(m2);
#pragma unroll
        for (int i = 0; i < 16; ++i) { ng0[i] = -m; ng1[i] = -m2; o0[i] = 0.f; o1[i] = 0.f; }
        PIN(ng0); PIN(ng1);
      }
      asm volatile("s_nop 3");
      f32x16 sa = mfma_v(kf, q0, ng0), sb;
      asm volatile("s_nop 15");
      u32x4 kn = *(lds_u4p)(kq + 1024);
      u32x4 v0 = *(lds_u4p)vq, v1 = *(lds_u4p)(vq + VBLK);
      u32x4 p1 = {0, 0, 0, 0};
      const u32x4 sv4 = {SENT, SENT, SENT, SENT};
      const int fz = layer == 0 ? 0 : 0x40000000;
#define FILL(off, k) __builtin_amdgcn_raw_buffer_store_b128(sv4, rs, lo + (k) * 1024, sop(fz + (off)), AUX_SC1)
      auto side = [&](auto NC) {
        constexpr int n = decltype(NC)::value;
        if constexpr (n < 2) FILL(WS_HB + HB_X1 + own, n);
        else if constexpr (n < 10) { constexpr int j = n - 2; FILL(WS_HB + HB_H + ((j & 4) ? hob : hoa) + ((j >> 1) & 1) * KS_F * 1024, j & 1); }
        else if constexpr (n < 12) FILL(WS_X2 + own, n - 10);
        else if constexpr (n < 14) FILL(WS_HB + HB_STR + HB_O + own, n - 12);
        else if constexpr (n < 16) FILL(WS_HB + HB_STR + HB_X1 + own, n - 14);
        else if constexpr (n < 24) { constexpr int j = n - 16; FILL(WS_HB + HB_STR + HB_H + ((j & 4) ? hob : hoa) + ((j >> 1) & 1) * KS_F * 1024, j & 1); }
        else if constexpr (n < 27) { constexpr int j = n - 24;
          __builtin_amdgcn_raw_buffer_store_b128(sv4, rs, lane < 16 ? lo : 0x30000000, sop(fz + (j == 0 ? WS_HB + HB_ST1 : j == 1 ? WS_ST2 : WS_HB + HB_STR + HB_ST1) + (tt * 16 + ft) * 256), AUX_SC1); }
        else if constexpr (n < 28) { }
        else if constexpr (n < 44) { constexpr int j = n - 28; wof[j & 1][j >> 1] = LDK(sop((j & 1) ? wob : woa), j >> 1); }
        else if constexpr (n < 76) w1a[n - 44] = LDK(sop(w1ao), n - 44);
        else if constexpr (n < 92) w1b[n - 76] = LDK(sop(w1bo), n - 76);
      };
#define SIDE(n) do { if constexpr ((n) >= 0) { side(std::integral_constant<int, (n) < 0 ? 0 : (n)>{}); side(std::integral_constant<int, (n) < 0 ? 0 : (n) + 1>{}); } } while (0)
#define SUB(sc, oc, sn, kx, qx, nx, op, LD1, LD2, LD3) do { u32x4 p0; \
    sc[0] = EX(sc[0]); sc[1] = EX(sc[1]); sc[2] = EX(sc[2]); sc[3] = EX(sc[3]); sc[4] = EX(sc[4]); SBAR(); \
    sn = mfma_v(kx, qx, nx); SBAR(); \
    LD1; SBAR(); \
    sc[5] = EX(sc[5]); sc[6] = EX(sc[6]); sc[7] = EX(sc[7]); sc[8] = EX(sc[8]); sc[9] = EX(sc[9]); SBAR(); \
    op = MFMA(v1, p1, op); SBAR(); \
    LD2; SBAR(); \
    p0[0] = pk2(sc[0], sc[1]); p0[1] = pk2(sc[2], sc[3]); p0[2] = pk2(sc[4], sc[5]); p0[3] = pk2(sc[6], sc[7]); SBAR(); \
    sc[10] = EX(sc[10]); sc[11] = EX(sc[11]); sc[12] = EX(sc[12]); sc[13] = EX(sc[13]); sc[14] = EX(sc[14]); sc[15] = EX(sc[15]); SBAR(); \
    oc = MFMA(v0, p0, oc); SBAR(); \
    LD3; SBAR(); \
    p1[0] = pk2(sc[8], sc[9]); p1[1] = pk2(sc[10], sc[11]); p1[2] = pk2(sc[12], sc[13]); p1[3] = pk2(sc[14], sc[15]); SBAR(); } while (0)
#define TILE(kcur, knext, koff, voff, N) do { \
    SUB(sa, o0, sb, kcur, q1, ng1, o1, SIDE(N), v1 = *(lds_u4p)(vq + (voff) + VBLK), SIDE((N) < 0 ? -1 : (N) + 2)); \
    SUB(sb, o1, sa, knext, q0, ng0, o0, kcur = *(lds_u4p)(kq + (koff)), SIDE((N) < 0 ? -1 : (N) + 4), v0 = *(lds_u4p)(vq + (voff) + 2 * VBLK)); } while (0)
#define TILE2(N) do { TILE(kf, kn, 2 * 1024, 0, N); TILE(kn, kf, 3 * 1024, 2 * VBLK, (N) < 0 ? -1 : (N) + 6); kq += 2048; vq += 4 * VBLK; } while (0)
#pragma unroll 1
      for (int t = 0; t < 8; t += 2) TILE2(-1);
      {
        unsigned m0 = 0;
#pragma unroll
        for (int i = 0; i < 24; ++i) m0 = fold4(m0, kb[i]);
        if (__any(m0 == SENT)) { HANDOFF_LOAD(24, kb, KVB(i), fold4(m_, kb[i])); }
#pragma unroll
        for (int i = 0; i < 12; ++i) { *(LAS u32x4*)(kdst + (16 + 12 * w + i) * 1024) = kb[2 * i]; *(LAS u32x4*)(vdst + (16 + 12 * w + i) * 2 * VBLK) = kb[2 * i + 1]; }
      }
      BAR();
      STAMP(KID, 13);
      TILE2(0); TILE2(12); TILE2(24); TILE2(36); TILE2(48); TILE2(60); TILE2(72); TILE2(84);
#pragma unroll 1
      for (int t = 24; t < 64; t += 2) TILE2(-1);
      o1 = MFMA(v1, p1, o1);
      STAMP(KID, 14);
      float l0 = lowhalf(o0[8]), l1 = lowhalf(o1[8]);
      bool bad = !(l0 < 1.0e18f) || !(l1 < 1.0e18f);
#pragma unroll
      for (int i = 0; i < 8; ++i) bad |= !(__builtin_fabsf(o0[i]) < 3.0e38f) || !(__builtin_fabsf(o1[i]) < 3.0e38f);
      if (__builtin_expect(__any(bad), 0)) {
        attn_simple(kp0, vp0, q0, o0);
        attn_simple(kp0, vp0, q1, o1);
        l0 = lowhalf(o0[8]); l1 = lowhalf(o1[8]);
      }
      const float r0 = 1.0f / l0, r1 = 1.0f / l1;
      const u32x4 oa = {pk2(o0[0] * r0, o0[1] * r0), pk2(o0[2] * r0, o0[3] * r0), pk2(o0[4] * r0, o0[5] * r0), pk2(o0[6] * r0, o0[7] * r0)};
      const u32x4 ob = {pk2(o1[0] * r1, o1[1] * r1), pk2(o1[2] * r1, o1[3] * r1), pk2(o1[4] * r1, o1[5] * r1), pk2(o1[6] * r1, o1[7] * r1)};
      asm volatile("s_waitcnt vmcnt(0)" ::: "memory");
      { const int oo_ = sop(hb + HB_O + own); st16c(rs, oa, lo, oo_); st16c(rs, ob, lo + 1024, oo_); }
      SBAR();
    }
    STAMP(KID, 15);
    u32x4 w2f[2][22];
#define W2SLOT(j) ((j) < 8 ? RED_BYTES + w * 8192 + (j) * 1024 : W2L_HI + w * 4096 + ((j) - 8) * 1024)
#define W2DMA(j) glds16(ws + (((j) & 1) ? w2b : w2a) + (22 + ((j) >> 1)) * 1024 + lo, lds0 + W2SLOT(j))
#define W2LOAD(n) w2f[(n) & 1][(n) >> 1] = LDK(sop(((n) & 1) ? w2b : w2a), ord28((n) >> 1))
    RELANE();
#pragma unroll
    for (int n = 16; n < 32; ++n) w1b[n] = LDK(sop(w1bo), n);
    SBAR();
    {
      u32x4 af[16];
      const int aa_ = sop(hb + HB_O + (tt * KS_D + 8 * w) * 1024), ab_ = sop(hb + HB_O + (tto * KS_D + 8 * w) * 1024);
      HANDOFF_LOAD(16, af, LDKC(i < 8 ? ab_ : aa_, i & 7), fold4(m_, af[i]));
      STAMP(KID, 1);
      BAR();
      f32x16 o = unpack16(*(lds_u4p)((lcp)L + RES_OFF + w * 2048 + lo), *(lds_u4p)((lcp)L + RES_OFF + w * 2048 + 1024 + lo));
#pragma unroll
      for (int qp = 1; qp < 4; ++qp) {
        f32x16 a = {};
#pragma unroll
        for (int i = 0; i < 8; ++i) {
          a = MFMA(wof[qp >> 1][i], af[(qp & 1) ? i : 8 + i], a);
          if (i & 1) W2DMA(4 * (qp - 1) + (i >> 1));
        }
        red_put(a, red, w, qp, lane);
        SBAR();
      }
#pragma unroll
      for (int i = 0; i < 8; ++i) {
        o = MFMA(wof[0][i], af[8 + i], o);
        if (i & 1) W2LOAD(i >> 1);
      }
      BAR();
      red_get(o, red, w, lane);
      u32x4 y0, y1;
      publish_x(rs, o, hb + HB_X1, hb + HB_ST1, tt, ft, lane, y0, y1);
      SBAR();
    }
    STAMP(KID, 2);
    RELANE();
    f32x4 csa[4], csb[4];
#pragma unroll
    for (int n = 4; n < 16; ++n) W2LOAD(n);
    SBAR();
    STAMP(KID, 3);
    {
      u32x4 xf[16];
      const int xo_ = sop(hb + HB_X1 + ((2 * tp) * KS_D + 16 * w) * 1024);
      HANDOFF_LOAD(16, xf, LDKC(xo_, i), fold4(m_, xf[i]));
#pragma unroll
      for (int i = 0; i < 16; ++i) *(LAS u32x4*)(L + CH_X + (16 * w + i) * 1024 + lo) = xf[i];
      BAR();
    }
    STAMP(KID, 4);
    u32x4 hf[7][8];
    const int ha_ = hb + HB_H + (tt * KS_F + 28 * w) * 1024, hb_ = hb + HB_H + (tto * KS_F + 28 * w) * 1024;
#define HLOAD(c, i) LDKC(sop(((i) & 1) ? hb_ : ha_), ord28(4 * (c) + ((i) >> 1)))
    {
      const lcp xp = (lcp)L + CH_X + lo;
      u32x2 st0[8], st1[8];
#define STLOAD(t, i) ld8c(rs, (8 * h * 32 + r) * 8 + (i) * 256, hb + HB_ST1 + (2 * tp + (t)) * 16 * 256)
      auto epi = [&](const f32x16& o, const f32x4 (&cs)[4], f32x2 ms, int hoff) {
        const float nm = -ms[0], sc = ms[1];
        u32x4 e0, e1;
#pragma unroll
        for (int g = 0; g < 4; ++g) {
          const float a = __builtin_fmaxf(__builtin_fmaf(nm, cs[g][0], o[4 * g]) * sc, 0.f), b = __builtin_fmaxf(__builtin_fmaf(nm, cs[g][1], o[4 * g + 1]) * sc, 0.f);
          const float c = __builtin_fmaxf(__builtin_fmaf(nm, cs[g][2], o[4 * g + 2]) * sc, 0.f), d = __builtin_fmaxf(__builtin_fmaf(nm, cs[g][3], o[4 * g + 3]) * sc, 0.f);
          if (g < 2) { e0[2 * g] = pk2(a, b); e0[2 * g + 1] = pk2(c, d); }
          else { e1[2 * (g - 2)] = pk2(a, b); e1[2 * (g - 2) + 1] = pk2(c, d); }
        }
        const int eo_ = sop(hb + HB_H + hoff);
        st16c(rs, e0, lo, eo_); st16c(rs, e1, lo + 1024, eo_);
      };
      constexpr int PF = 2;
      u32x4 xa[PF], xb[PF];
#pragma unroll
      for (int i = 0; i < PF; ++i) { xa[i] = *(lds_u4p)(xp + i * 1024); xb[i] = *(lds_u4p)(xp + (32 + i) * 1024); }
      f32x16 a0 = {}, a1 = {};
#pragma unroll
      for (int i = 0; i < 32; ++i) {
        const u32x4 x0 = xa[i % PF], x1 = xb[i % PF];
        a0 = MFMA(w1a[i], x0, a0);
        a1 = MFMA(w1a[i], x1, a1);
        const int n = (i + PF) & 31;
        xa[i % PF] = *(lds_u4p)(xp + n * 1024); xb[i % PF] = *(lds_u4p)(xp + (32 + n) * 1024);
        if (i >= 8 && i < 28) W2LOAD(8 + i);
        if (i >= 8 && i < 16) { st0[i - 8] = STLOAD(0, i - 8); st1[i - 8] = STLOAD(1, i - 8); }
        if (i >= 16 && i < 20) csa[i - 16] = __builtin_bit_cast(f32x4, ld16(rs, h * 16 + (i - 16) * 32, sop(WS_CS1 + 128 * fta)));
        SBAR();
      }
      PINA(a0); PINA(a1);
      {
        unsigned m0 = 0;
#pragma unroll
        for (int i = 0; i < 8; ++i) m0 = umax3(umax3(m0, st0[i][0], st0[i][1]), st1[i][0], st1[i][1]);
        if (__any(m0 == SENT)) {
          HANDOFF_LOAD(8, st0, STLOAD(0, i), umax3(m_, st0[i][0], st0[i][1]));
          HANDOFF_LOAD(8, st1, STLOAD(1, i), umax3(m_, st1[i][0], st1[i][1]));
        }
      }
      f32x2 sf0[8], sf1[8];
#pragma unroll
      for (int i = 0; i < 8; ++i) { sf0[i] = __builtin_bit_cast(f32x2, st0[i]); sf1[i] = __builtin_bit_cast(f32x2, st1[i]); }
      const f32x2 ms0 = combine_stats(sf0), ms1 = combine_stats(sf1);
      STAMP(KID, 11);
      f32x16 b0 = {}, b1 = {};
#pragma unroll
      for (int i = 0; i < 32; ++i) {
        const u32x4 x0 = xa[i % PF], x1 = xb[i % PF];
        b0 = MFMA(w1b[i], x0, b0);
        b1 = MFMA(w1b[i], x1, b1);
        if (i + PF < 32) { xa[i % PF] = *(lds_u4p)(xp + (i + PF) * 1024); xb[i % PF] = *(lds_u4p)(xp + (32 + i + PF) * 1024); }
        if (i == 0) epi(a0, csa, ms0, hoa);
        if (i == 4) epi(a1, csa, ms1, hoa + KS_F * 1024);
        if (i >= 4 && i < 12) W2LOAD(32 + i);
        if (i >= 8 && i < 12) csb[i - 8] = __builtin_bit_cast(f32x4, ld16(rs, h * 16 + (i - 8) * 32, sop(WS_CS1 + 128 * ftb)));
        if (i >= 16) { hf[(i - 16) >> 2][2 * (i & 3)] = HLOAD((i - 16) >> 2, 2 * (i & 3)); hf[(i - 16) >> 2][2 * (i & 3) + 1] = HLOAD((i - 16) >> 2, 2 * (i & 3) + 1); }
        if (i >= 8 || (i & 3) == 3) SBAR();
      }
      PINA(b0); PINA(b1);
      if (w != 3) epi(b0, csb, ms0, hob);
      if (w != 2) epi(b1, csb, ms1, hob + KS_F * 1024);
      SBAR();
    }
    STAMP(KID, 5);
    RELANE();
    u32x4 wq[2][8], wkv[8];
    f32x16 o;
    {
      f32x16 acc[4];
      u32x4 w2l[12];
#define W2F(a, pos) ((pos) < 22 ? w2f[a][(pos) < 22 ? (pos) : 0] : w2l[(pos) < 22 ? 0 : 2 * ((pos) - 22) + (a)])
      {
        const lcp yp = (lcp)L + CH_X + (ti * 32 + 2 * ft) * 1024 + lo;
        acc[0] = unpack16(*(lds_u4p)yp, *(lds_u4p)(yp + 1024));
      }
      acc[1] = f32x16{}; acc[2] = f32x16{}; acc[3] = f32x16{};
      const int wqa = WS_WQKV + (ft * KS_D + 8 * w) * 1024, wqb = WS_WQKV + (fto * KS_D + 8 * w) * 1024;
#pragma unroll
      for (int c = 0; c < 7; ++c) {
        {
          unsigned m0 = 0;
#pragma unroll
          for (int i = 0; i < 8; ++i) m0 = fold4(m0, hf[c][i]);
          if (__any(m0 == SENT)) { HANDOFF_LOAD(8, hf[c], HLOAD(c, i), fold4(m_, hf[c][i])); }
        }
        if (c < 6) {
#pragma unroll
          for (int i = 0; i < 4; ++i) {
#pragma unroll
            for (int qp = 0; qp < 4; ++qp) acc[qp] = MFMA(W2F(qp >> 1, 4 * c + i), hf[c][2 * i + (qp & 1)], acc[qp]);
            if (c == 4) { w2l[3 * i] = *(lds_u4p)((lcp)L + W2SLOT(3 * i) + lo); w2l[3 * i + 1] = *(lds_u4p)((lcp)L + W2SLOT(3 * i + 1) + lo); w2l[3 * i + 2] = *(lds_u4p)((lcp)L + W2SLOT(3 * i + 2) + lo); }
            if (c >= 1 && c < 4) { hf[c + 3][2 * i] = HLOAD(c + 3, 2 * i); hf[c + 3][2 * i + 1] = HLOAD(c + 3, 2 * i + 1); }
            if (c >= 4) { wq[0][4 * (c - 4) + i] = LDK(sop(wqa), 4 * (c - 4) + i); wq[1][4 * (c - 4) + i] = LDK(sop(wqb), 4 * (c - 4) + i); }
            SBAR();
          }
          PINA(acc[0]); PINA(acc[1]); PINA(acc[2]); PINA(acc[3]);
        } else {
#pragma unroll
          for (int qq = 1; qq < 5; ++qq) {
            const int qp = qq & 3;
#pragma unroll
            for (int i = 0; i < 4; ++i) acc[qp] = MFMA(W2F(qp >> 1, 4 * c + i), hf[c][2 * i + (qp & 1)], acc[qp]);
            if (qp) red_put(acc[qp], red, w, qp, lane);
            SBAR();
          }
        }
      }
      STAMP(KID, 7);
      BAR();
      RELANE();
      o = acc[0];
      red_get(o, red, w, lane);
    }
    if (layer == 1) {
      u32x4 z0, z1;
#pragma unroll
      for (int i = 0; i < 4; ++i) { z0[i] = pk2(o[2 * i], o[2 * i + 1]); z1[i] = pk2(o[8 + 2 * i], o[8 + 2 * i + 1]); }
      unsigned short* yp = out + (size_t)(32 * tt + r) * D_ + 32 * ft + 4 * h;
      u32x2 a0 = {z0[0], z0[1]}, a1 = {z0[2], z0[3]}, a2 = {z1[0], z1[1]}, a3 = {z1[2], z1[3]};
      *(u32x2*)(yp) = a0; *(u32x2*)(yp + 8) = a1; *(u32x2*)(yp + 16) = a2; *(u32x2*)(yp + 24) = a3;
      STAMP(KID, 8);
      return;
    }
    {
      u32x4 res0, res1;
      publish_x(rs, o, WS_X2, WS_ST2, tt, ft, lane, res0, res1);
      *(LAS u32x4*)(L + RES_OFF + w * 2048 + lo) = res0; *(LAS u32x4*)(L + RES_OFF + w * 2048 + 1024 + lo) = res1;
    }
    SBAR();
    f32x4 cs[4], csk[2];
#pragma unroll
    for (int i = 0; i < 8; ++i) wkv[i] = LDK(sop(fp < 2 ? WS_WQKV + (16 * KS_D + 8 * w) * 1024 : 0x40000000), i);
#pragma unroll
    for (int g = 0; g < 4; ++g) cs[g] = __builtin_bit_cast(f32x4, ld16(rs, h * 16 + g * 32, WS_CSQKV + 128 * ft));
#pragma unroll
    for (int g = 0; g < 2; ++g) csk[g] = __builtin_bit_cast(f32x4, ld16(rs, h * 16 + g * 32, WS_CSQKV + 2048));
    const float csv = __builtin_bit_cast(float, __builtin_amdgcn_raw_buffer_load_b32(rs, r * 4, WS_CSQKV + 2048, 0));
    SBAR();
    STAMP(KID, 8);
    {
      u32x4 xf[2][8];
      u32x2 st[8];
      const int xa_ = sop(WS_X2 + (tt * KS_D + 8 * w) * 1024), xb_ = sop(WS_X2 + (tto * KS_D + 8 * w) * 1024);
      int spins = 0;
      for (;;) {
        asm volatile("" ::: "memory");
#pragma unroll
        for (int i = 0; i < 8; ++i) { xf[0][i] = LDKC(xa_, i); xf[1][i] = LDKC(xb_, i); }
#pragma unroll
        for (int i = 0; i < 8; ++i) st[i] = ld8c(rs, (8 * h * 32 + r) * 8 + i * 256, WS_ST2 + tt * 16 * 256);
        unsigned m = 0;
#pragma unroll
        for (int i = 0; i < 8; ++i) m = umax3(fold4(fold4(m, xf[0][i]), xf[1][i]), st[i][0], st[i][1]);
        if (!__any(m == SENT) || ++spins > SPIN_CAP) break;
      }
      f32x2 sf[8];
#pragma unroll
      for (int i = 0; i < 8; ++i) sf[i] = __builtin_bit_cast(f32x2, st[i]);
      const f32x2 ms = combine_stats(sf);
      STAMP(KID, 9);
      BAR();
      if (fp < 2) qkv_finish<true>(rs, L, wq, xf, wkv, ms, cs, csk, csv, tp, fp, w, lane, WS_KV + KV_STR, q0, q1);
      else qkv_finish<false>(rs, L, wq, xf, wkv, ms, cs, csk, csv, tp, fp, w, lane, WS_KV + KV_STR, q0, q1);
      STAMP(KID, 10);
      BAR();
    }
  }
}

extern "C" void kernel_launch(void* const* d_in, const int* in_sizes, int n_in, void* d_out, int out_size, void* d_ws, size_t ws_size,
                              hipStream_t stream) {
  const unsigned short* x = (const unsigned short*)d_in[0];
  const unsigned short* wq = (const unsigned short*)d_in[1];
  const unsigned short* wk = (const unsigned short*)d_in[2];
  const unsigned short* wv = (const unsigned short*)d_in[3];
  const unsigned short* wo = (const unsigned short*)d_in[4];
  const unsigned short* w1 = (const unsigned short*)d_in[6];
  const unsigned short* w2 = (const unsigned short*)d_in[7];
  char* ws = (char*)d_ws;
  (void)hipFuncSetAttribute((const void*)qkv_kernel, hipFuncAttributeMaxDynamicSharedMemorySize, QKV_LDS);
  (void)hipFuncSetAttribute((const void*)main_kernel, hipFuncAttributeMaxDynamicSharedMemorySize, MAIN_LDS);
  prep_kernel<<<1024, 256, 0, stream>>>(x, wq, wk, wv, wo, w1, w2, ws);
  qkv_kernel<<<256, 256, QKV_LDS, stream>>>(ws);
  main_kernel<<<256, 256, MAIN_LDS, stream>>>(ws, (unsigned short*)d_out);
}
```

```cpp
#include <hip/hip_runtime.h>
#include <hip/hip_bf16.h>
#include <stdint.h>
#include <type_traits>

typedef __attribute__((ext_vector_type(8))) __bf16 bf16x8;
typedef __attribute__((ext_vector_type(2))) __bf16 bf16x2;
typedef __attribute__((ext_vector_type(16))) float f32x16;
typedef __attribute__((ext_vector_type(4))) float f32x4;
typedef __attribute__((ext_vector_type(2))) float f32x2;
typedef __attribute__((ext_vector_type(4))) unsigned u32x4;
typedef __attribute__((ext_vector_type(2))) unsigned u32x2;
#define DI __device__ __forceinline__
#define MFMA(a, b, c) __builtin_amdgcn_mfma_f32_32x32x16_bf16(__builtin_bit_cast(bf16x8, (a)), __builtin_bit_cast(bf16x8, (b)), (c), 0, 0, 0)
#define SBAR() __builtin_amdgcn_sched_barrier(0)
#define PIN(x) asm volatile("" : "+v"(x))
#define PINA(x) asm volatile("" : "+a"(x))
#define LAS __attribute__((address_space(3)))
#define GAS __attribute__((address_space(1)))

constexpr int S_ = 2048, D_ = 512, DFF = 1792;
constexpr int NTT = S_ / 32;
constexpr int KS_D = D_ / 16;
constexpr int KS_F = DFF / 16;
constexpr int VBLK = 544;
constexpr int KIMG_BYTES = NTT * 1024;
constexpr int VIMG_BYTES = NTT * 2 * VBLK;
constexpr float QSCALE = 0.25f * 1.4426950408889634f;

constexpr int MB2 = 2 * 1024 * 1024;
constexpr int WS_WQKV = 0;
constexpr int WS_WO   = WS_WQKV + 17 * 32 * 1024;
constexpr int WS_W1   = WS_WO + 16 * 32 * 1024;
constexpr int WS_W2   = WS_W1 + 56 * 32 * 1024;
constexpr int WS_CSQKV = WS_W2 + 16 * 112 * 1024;
constexpr int WS_CS1  = WS_CSQKV + 4096;
constexpr int WS_Q    = WS_CS1 + 16384;
constexpr int WS_KV   = WS_Q + MB2;
constexpr int KV_STR  = 128 * 1024;
constexpr int HB_O = 0, HB_X1 = MB2, HB_ST1 = 2 * MB2, HB_H = HB_ST1 + 512 * 1024, HB_STR = HB_H + S_ * DFF * 2;
constexpr int WS_HB   = WS_KV + 2 * KV_STR;
constexpr int WS_X2   = WS_HB + 2 * HB_STR;
constexpr int WS_ST2  = WS_X2 + MB2;
constexpr int WS_END  = WS_ST2 + 512 * 1024;

#define STAMP(k, i)

DI unsigned pk2(float a, float b) { f32x2 v = {a, b}; bf16x2 r = __builtin_convertvector(v, bf16x2); return __builtin_bit_cast(unsigned, r); }
DI float bflo(unsigned u) { return __uint_as_float(u << 16); }
DI float bfhi(unsigned u) { return __uint_as_float(u & 0xffff0000u); }
DI float xsum(float v) {
  auto rr = __builtin_amdgcn_permlane32_swap(__float_as_uint(v), __float_as_uint(v), false, false);
  return __uint_as_float(rr[0]) + __uint_as_float(rr[1]);
}
DI float xmax(float v) {
  auto rr = __builtin_amdgcn_permlane32_swap(__float_as_uint(v), __float_as_uint(v), false, false);
  return __builtin_fmaxf(__uint_as_float(rr[0]), __uint_as_float(rr[1]));
}
DI float lowhalf(float v) {
  auto rr = __builtin_amdgcn_permlane32_swap(__float_as_uint(v), __float_as_uint(v), false, false);
  return __uint_as_float(rr[0]);
}
DI int perm16(int h, int j) { return 8 * (j >> 2) + 4 * h + (j & 3); }

typedef __amdgpu_buffer_rsrc_t rsrc_t;
constexpr int AUX_SC1 = 16;
DI rsrc_t ws_rsrc(char* ws) { return __builtin_amdgcn_make_buffer_rsrc((void*)ws, 0, WS_END, 0x00020000); }
DI u32x4 ld16(rsrc_t rs, int voff, int soff) { return __builtin_amdgcn_raw_buffer_load_b128(rs, voff, soff, 0); }
DI u32x4 ld16c(rsrc_t rs, int voff, int soff) { return __builtin_amdgcn_raw_buffer_load_b128(rs, voff, soff, AUX_SC1); }
DI u32x2 ld8c(rsrc_t rs, int voff, int soff) { return __builtin_amdgcn_raw_buffer_load_b64(rs, voff, soff, AUX_SC1); }
#define ST_PAD(v) asm volatile("s_nop 1" :: "v"(v) : "memory")
DI void st16(rsrc_t rs, u32x4 v, int voff, int soff) { __builtin_amdgcn_raw_buffer_store_b128(v, rs, voff, soff, 0); ST_PAD(v); }
DI int sop(int x) { asm volatile("" : "+s"(x)); return x; }
#define LDK(base, k) ld16(rs, lo + ((k) & 3) * 1024, (base) + ((k) >> 2) * 4096)
#define LDKC(base, k) ld16c(rs, lo + ((k) & 3) * 1024, (base) + ((k) >> 2) * 4096)
constexpr unsigned SENT = 0xffffffffu;
DI unsigned clean(unsigned u) { return u < SENT ? u : SENT - 1u; }
DI void st16c(rsrc_t rs, u32x4 v, int voff, int soff) {
  u32x4 c = {clean(v[0]), clean(v[1]), clean(v[2]), clean(v[3])};
  __builtin_amdgcn_raw_buffer_store_b128(c, rs, voff, soff, AUX_SC1);
  ST_PAD(c);
}
DI void st8c(rsrc_t rs, f32x2 v, int voff, int soff) {
  u32x2 c = {clean(__float_as_uint(v[0])), clean(__float_as_uint(v[1]))};
  __builtin_amdgcn_raw_buffer_store_b64(c, rs, voff, soff, AUX_SC1);
}
DI unsigned umax3(unsigned a, unsigned b, unsigned c) { unsigned m = a > b ? a : b; return m > c ? m : c; }
DI unsigned fold4(unsigned m, u32x4 v) { return umax3(umax3(m, v[0], v[1]), v[2], v[3]); }
constexpr int SPIN_CAP = 1 << 15;

#define HANDOFF_LOAD(N, DST, LOADEXPR, FOLD) do { \
    int spins_ = 0; \
    for (;;) { \
      asm volatile("" ::: "memory"); \
      _Pragma("unroll") for (int i = 0; i < (N); ++i) DST[i] = LOADEXPR; \
      unsigned m_ = 0; \
      _Pragma("unroll") for (int i = 0; i < (N); ++i) m_ = FOLD; \
      if (!__any(m_ == SENT) || ++spins_ > SPIN_CAP) break; \
      __builtin_amdgcn_s_sleep(2); \
    } } while (0)

#define VERIFY(N, DST, LOADEXPR) do { \
    int spins_ = 0; \
    _Pragma("nounroll") for (;;) { \
      unsigned m_ = 0; \
      _Pragma("unroll") for (int i = 0; i < (N); ++i) m_ = fold4(m_, DST[i]); \
      if (!__any(m_ == SENT) || ++spins_ > SPIN_CAP) break; \
      __builtin_amdgcn_s_sleep(1); \
      asm volatile("" ::: "memory"); \
      _Pragma("unroll") for (int i = 0; i < (N); ++i) DST[i] = LOADEXPR; \
    } } while (0)

#define BAR() do { asm volatile("s_waitcnt lgkmcnt(0)" ::: "memory"); __builtin_amdgcn_s_barrier(); asm volatile("" ::: "memory"); } while (0)

typedef LAS const u32x4* lds_u4p;
typedef const LAS char* lcp;
typedef short s16x4 __attribute__((ext_vector_type(4)));
DI void glds16(const void* g, unsigned lds_base) {
  unsigned sv;
  asm volatile("s_mov_b32 %0, m0\n\ts_mov_b32 m0, %2\n\ts_nop 0\n\tglobal_load_lds_dwordx4 %1, off\n\ts_mov_b32 m0, %0"
               : "=&s"(sv) : "v"(g), "s"(lds_base) : "memory");
}
DI u32x2 ldtr(lcp p) { return __builtin_bit_cast(u32x2, __builtin_amdgcn_ds_read_tr16_b64_v4i16((LAS s16x4*)p)); }
DI u32x4 tr_frag(lcp p, int rowbytes, int s) {
  const u32x2 a = ldtr(p + 16 * s * rowbytes), b = ldtr(p + (16 * s + 8) * rowbytes);
  u32x4 o = {a[0], a[1], b[0], b[1]};
  return o;
}
constexpr int K1_X = 0, K1_W = 65536, K1_S = 131072, K1_LDS = 163840;
#define K1_AUX 16
DI void st16k(rsrc_t rs, u32x4 v, int voff, int soff) { __builtin_amdgcn_raw_buffer_store_b128(v, rs, voff, soff, K1_AUX); ST_PAD(v); }
DI float dot2(unsigned a, unsigned b, float c) { return __builtin_amdgcn_fdot2_f32_bf16(__builtin_bit_cast(bf16x2, a), __builtin_bit_cast(bf16x2, b), c, false); }
DI void put4(const f32x16& a, LAS char* p, int n) {
#pragma unroll
  for (int g = 0; g < 4; ++g) if (g < n) { f32x4 v = {a[4 * g], a[4 * g + 1], a[4 * g + 2], a[4 * g + 3]}; *(LAS f32x4*)(p + g * 1024) = v; }
}

__global__ void __launch_bounds__(512) first_kernel(const unsigned short* x, const unsigned short* wq, const unsigned short* wk,
                                                    const unsigned short* wv, const unsigned short* wo, const unsigned short* w1,
                                                    const unsigned short* w2, char* ws) {
  extern __shared__ __attribute__((aligned(16))) char smem[];
  LAS char* L = (LAS char*)smem;
  const unsigned lds0 = (unsigned)(uintptr_t)smem;
  const rsrc_t rs = ws_rsrc(ws);
  const int lane = threadIdx.x & 63, r = lane & 31, h = lane >> 5, lo = lane * 16;
  const int w8 = __builtin_amdgcn_readfirstlane(threadIdx.x >> 6), w = w8 & 3;
  const int xcd = blockIdx.x & 7, idx = blockIdx.x >> 3;
  const int tp = 4 * xcd + (idx >> 3), fp = idx & 7;
  const bool kv = fp < 2;
  const int trq = (lane & 15) >> 2, trp = lane & 3, trcb = (lane >> 4) & 1;
  const u32x4 ones = {0x3f803f80u, 0x3f803f80u, 0x3f803f80u, 0x3f803f80u};
  const int jw = kv ? 192 : (fp - 2) * 32 + tp;
  STAMP(0, 0);
  if (w8 < 4) {
    {
      const unsigned short* wsrc = wq + (size_t)(128 * w + (lane >> 3)) * 512 + 64 * fp + (lane & 7) * 8;
      const int xr = lane >> 4;
      if (kv) {
#pragma unroll
        for (int i = 0; i < 4; ++i) {
          glds16(wk + (size_t)128 * w * 16 + i * 512 + lane * 8, lds0 + K1_S + w * 8192 + i * 1024);
          glds16(wv + (size_t)128 * w * 16 + i * 512 + lane * 8, lds0 + K1_S + w * 8192 + 4096 + i * 1024);
        }
      }
#pragma unroll
      for (int i = 0; i < 16; ++i) {
        const int row = 4 * i + xr;
        glds16(x + (size_t)(64 * tp + row) * 512 + 128 * w + (((lane & 15) ^ (row & 15)) * 8), lds0 + K1_X + w * 16384 + i * 1024);
      }
#pragma unroll
      for (int i = 0; i < 16; ++i) glds16(wsrc + (size_t)i * 8 * 512, lds0 + K1_W + w * 16384 + i * 1024);
    }
    asm volatile("s_waitcnt vmcnt(16)" ::: "memory");
    u32x4 wf[2][8], xf[2][8];
    float s1[2] = {0.f, 0.f}, s2[2] = {0.f, 0.f};
#pragma unroll
    for (int b = 0; b < 2; ++b) {
      const lcp xp = (lcp)L + K1_X + w * 16384 + (32 * b + r) * 256 + h * 8;
#pragma unroll
      for (int s = 0; s < 8; ++s) {
        const u32x2 a = *(const LAS u32x2*)(xp + (((2 * s) ^ (r & 15)) * 16)), c = *(const LAS u32x2*)(xp + (((2 * s + 1) ^ (r & 15)) * 16));
        xf[b][s] = (u32x4){a[0], a[1], c[0], c[1]};
      }
    }
    f32x16 aV = {}, aK = {}, c2 = {};
    if (kv) {
      const lcp kp = (lcp)L + K1_S + w * 8192 + trcb * 4096 + (4 * h + trq) * 32 + trp * 8;
#pragma unroll
      for (int s = 0; s < 8; ++s) {
        const u32x4 wkv = tr_frag(kp, 32, s);
        const u32x4 b = fp ? xf[1][s] : xf[0][s];
        aV = MFMA(b, wkv, aV); aK = MFMA(wkv, b, aK); c2 = MFMA(wkv, ones, c2);
        if (tp == 2 && fp == 0) st16k(rs, wkv, lo, WS_WQKV + (16 * KS_D + 8 * w + s) * 1024);
      }
    }
#pragma unroll
    for (int b = 0; b < 2; ++b)
#pragma unroll
      for (int s = 0; s < 8; ++s)
#pragma unroll
        for (int e = 0; e < 4; ++e) { s1[b] = dot2(xf[b][s][e], 0x3f803f80u, s1[b]); s2[b] = dot2(xf[b][s][e], xf[b][s][e], s2[b]); }
    asm volatile("s_waitcnt vmcnt(0)" ::: "memory");
    STAMP(0, 1);
    {
      const lcp wp = (lcp)L + K1_W + w * 16384 + (4 * h + trq) * 128 + (16 * trcb + 4 * trp) * 2;
#pragma unroll
      for (int s = 0; s < 8; ++s) { wf[0][s] = tr_frag(wp, 128, s); wf[1][s] = tr_frag(wp + 64, 128, s); }
    }
    LAS char* xa = L + K1_X + w * 16384 + lo;
    LAS char* wa = L + K1_W + w * 16384;
#pragma unroll
    for (int f = 0; f < 2; ++f) {
      f32x16 a0 = {}, a1 = {}, c = {};
#pragma unroll
      for (int s = 0; s < 8; ++s) { a0 = MFMA(wf[f][s], xf[0][s], a0); a1 = MFMA(wf[f][s], xf[1][s], a1); c = MFMA(wf[f][s], ones, c); }
      put4(a0, xa + (2 * f) * 4096, 4); put4(a1, xa + (2 * f + 1) * 4096, 4);
      if (f == 0) asm volatile("s_waitcnt lgkmcnt(0)" ::: "memory");
      if (r == 0) {
#pragma unroll
        for (int g = 0; g < 4; ++g) { f32x4 v = {c[4 * g], c[4 * g + 1], c[4 * g + 2], c[4 * g + 3]}; *(LAS f32x4*)(wa + 6144 + f * 128 + g * 32 + h * 16) = v; }
      }
    }
    if (kv) {
      put4(aV, wa + lo, 4); put4(aK, wa + 4096 + lo, 2);
      if (r == 0) {
#pragma unroll
        for (int g = 0; g < 4; ++g) { f32x4 v = {c2[4 * g], c2[4 * g + 1], c2[4 * g + 2], c2[4 * g + 3]}; *(LAS f32x4*)(wa + 6144 + 256 + g * 32 + h * 16) = v; }
      }
    }
#pragma unroll
    for (int b = 0; b < 2; ++b) { const f32x2 sv = {s1[b], s2[b]}; *(LAS f32x2*)(wa + 7168 + b * 512 + lane * 8) = sv; }
    if (tp == 1) {
#pragma unroll
      for (int s = 0; s < 8; ++s) {
        st16k(rs, wf[0][s], lo, WS_WQKV + ((2 * fp) * KS_D + 8 * w + s) * 1024);
        st16k(rs, wf[1][s], lo, WS_WQKV + ((2 * fp + 1) * KS_D + 8 * w + s) * 1024);
      }
    }
  } else {
    const unsigned short* src = w1; int ldw = DFF, k0 = 0, c0 = 0, dst = 0, ksn = KS_D;
    if (jw < 56) { src = w1; ldw = DFF; k0 = 256 * (jw & 1) + 64 * w; c0 = 64 * (jw >> 1); dst = WS_W1; ksn = KS_D; }
    else if (jw < 72) { const int b = (jw - 56) * 4 + w; src = wo; ldw = 512; k0 = 64 * (b & 7); c0 = 64 * (b >> 3); dst = WS_WO; ksn = KS_D; }
    else { const int b = (jw - 72) * 4 + w; src = w2; ldw = 512; k0 = 64 * (b >> 3); c0 = 64 * (b & 7); dst = WS_W2; ksn = KS_F; }
    if (jw < 128) {
      const unsigned short* p = src + (size_t)(k0 + (lane >> 3)) * ldw + c0 + (lane & 7) * 8;
#pragma unroll
      for (int i = 0; i < 8; ++i) glds16(p + (size_t)i * 8 * ldw, lds0 + K1_S + w * 8192 + i * 1024);
      asm volatile("s_waitcnt vmcnt(0)" ::: "memory");
      f32x16 c0a = {}, c1a = {};
      const lcp wp = (lcp)L + K1_S + w * 8192 + (4 * h + trq) * 128 + (16 * trcb + 4 * trp) * 2;
      const int d0 = dst + ((c0 >> 5) * ksn + (k0 >> 4)) * 1024;
      u32x4 a[4], b[4];
#pragma unroll
      for (int s = 0; s < 4; ++s) { a[s] = tr_frag(wp, 128, s); b[s] = tr_frag(wp + 64, 128, s); }
#pragma unroll
      for (int s = 0; s < 4; ++s) { st16k(rs, a[s], lo + s * 1024, d0); st16k(rs, b[s], lo + s * 1024, d0 + ksn * 1024); }
      if (jw < 56) {
#pragma unroll
        for (int s = 0; s < 4; ++s) { c0a = MFMA(a[s], ones, c0a); c1a = MFMA(b[s], ones, c1a); }
        if (r == 0) {
          LAS char* cp = L + K1_S + w * 8192 + h * 16;
#pragma unroll
          for (int g = 0; g < 4; ++g) {
            f32x4 u = {c0a[4 * g], c0a[4 * g + 1], c0a[4 * g + 2], c0a[4 * g + 3]}, v = {c1a[4 * g], c1a[4 * g + 1], c1a[4 * g + 2], c1a[4 * g + 3]};
            *(LAS f32x4*)(cp + g * 32) = u; *(LAS f32x4*)(cp + 128 + g * 32) = v;
          }
        }
      }
    }
    {
      const u32x4 sv = {SENT, SENT, SENT, SENT};
      const int gw = blockIdx.x * 4 + w;
      st16k(rs, sv, lo, WS_HB + HB_O + gw * 2048); st16k(rs, sv, lo + 1024, WS_HB + HB_O + gw * 2048);
      if (gw < 128) st16k(rs, sv, lo, WS_KV + KV_STR + gw * 1024);
    }
  }
  BAR();
  STAMP(0, 2);
  const int kq = w8 >> 1, ti = w8 & 1;
  float mean, rstd;
  {
    float s1 = 0.f, s2 = 0.f;
#pragma unroll
    for (int sw = 0; sw < 4; ++sw) { const f32x2 v = *(const LAS f32x2*)(L + K1_W + sw * 16384 + 7168 + ti * 512 + lane * 8); s1 += v[0]; s2 += v[1]; }
    s1 = xsum(s1); s2 = xsum(s2);
    mean = s1 * (1.0f / 512.0f);
    float var = __builtin_fmaf(-mean, mean, s2 * (1.0f / 512.0f));
    if (__builtin_expect(__any(!(mean * mean <= 64.0f * var)), 0)) {
      const unsigned short* xp = x + (size_t)(64 * tp + 32 * ti + r) * 512 + 256 * h;
      float m2 = 0.f;
      for (int i = 0; i < 32; ++i) {
        const u32x4 v = *(const u32x4*)(xp + i * 8);
#pragma unroll
        for (int e = 0; e < 4; ++e) { const float a = bflo(v[e]) - mean, b = bfhi(v[e]) - mean; m2 = __builtin_fmaf(a, a, m2); m2 = __builtin_fmaf(b, b, m2); }
      }
      var = xsum(m2) * (1.0f / 512.0f);
    }
    rstd = 1.0f / __builtin_sqrtf(var + 1e-5f);
  }
  if (kv && ti == fp && kq < 3) {
    const int kvo = WS_KV + (2 * tp + fp) * 2048;
    if (kq == 2) {
      float k[8];
#pragma unroll
      for (int g = 0; g < 2; ++g) {
        f32x4 s = {0.f, 0.f, 0.f, 0.f}, c = {0.f, 0.f, 0.f, 0.f};
#pragma unroll
        for (int sw = 0; sw < 4; ++sw) { s += *(const LAS f32x4*)(L + K1_W + sw * 16384 + 4096 + g * 1024 + lo); c += *(const LAS f32x4*)(L + K1_W + sw * 16384 + 6144 + 256 + g * 32 + h * 16); }
#pragma unroll
        for (int e = 0; e < 4; ++e) k[4 * g + e] = __builtin_fmaf(-mean, c[e], s[e]) * rstd;
      }
      const u32x4 ko = {clean(pk2(k[0], k[1])), clean(pk2(k[2], k[3])), clean(pk2(k[4], k[5])), clean(pk2(k[6], k[7]))};
      st16k(rs, ko, lo, kvo);
    } else {
      LAS char* tb = L + K1_S + kq * 512;
      if (h == 0) { const f32x2 m = {mean, rstd}; *(LAS f32x2*)(tb + r * 8) = m; }
      float csv = 0.f;
#pragma unroll
      for (int sw = 0; sw < 4; ++sw) csv += *(const LAS float*)(L + K1_W + sw * 16384 + 6144 + 256 + r * 4);
      if (tp == 2 && fp == 0 && kq == 0 && h == 0) __builtin_amdgcn_raw_buffer_store_b32(__float_as_uint(csv), rs, r * 4, WS_CSQKV + 2048, K1_AUX);
      float v[8];
#pragma unroll
      for (int g2 = 0; g2 < 2; ++g2) {
        f32x4 s = {0.f, 0.f, 0.f, 0.f};
#pragma unroll
        for (int sw = 0; sw < 4; ++sw) s += *(const LAS f32x4*)(L + K1_W + sw * 16384 + (2 * kq + g2) * 1024 + lo);
        const lcp mp = (lcp)tb + (16 * kq + 8 * g2 + 4 * h) * 8;
        const f32x4 m01 = *(const LAS f32x4*)mp, m23 = *(const LAS f32x4*)(mp + 16);
        v[4 * g2] = __builtin_fmaf(-m01[0], csv, s[0]) * m01[1]; v[4 * g2 + 1] = __builtin_fmaf(-m01[2], csv, s[1]) * m01[3];
        v[4 * g2 + 2] = __builtin_fmaf(-m23[0], csv, s[2]) * m23[1]; v[4 * g2 + 3] = __builtin_fmaf(-m23[2], csv, s[3]) * m23[3];
      }
      const u32x4 vo = {clean(pk2(v[0], v[1])), clean(pk2(v[2], v[3])), clean(pk2(v[4], v[5])), clean(pk2(v[6], v[7]))};
      st16k(rs, vo, r >= 16 ? 1024 + kq * 512 + h * 256 + (r - 16) * 16 : 0x30000000, kvo);
    }
  }
  {
    const int fi = kq >> 1, sp = kq & 1;
    u32x4 qf;
    const float sc = rstd * QSCALE;
#pragma unroll
    for (int g = 0; g < 2; ++g) {
      f32x4 s = {0.f, 0.f, 0.f, 0.f}, c = {0.f, 0.f, 0.f, 0.f};
#pragma unroll
      for (int sw = 0; sw < 4; ++sw) {
        s += *(const LAS f32x4*)(L + K1_X + sw * 16384 + (2 * fi + ti) * 4096 + (2 * sp + g) * 1024 + lo);
        c += *(const LAS f32x4*)(L + K1_W + sw * 16384 + 6144 + fi * 128 + (2 * sp + g) * 32 + h * 16);
      }
      qf[2 * g] = pk2(__builtin_fmaf(-mean, c[0], s[0]) * sc, __builtin_fmaf(-mean, c[1], s[1]) * sc);
      qf[2 * g + 1] = pk2(__builtin_fmaf(-mean, c[2], s[2]) * sc, __builtin_fmaf(-mean, c[3], s[3]) * sc);
      if (tp == 1 && ti == 0 && r == 0) st16k(rs, __builtin_bit_cast(u32x4, c), h * 16 + g * 32, WS_CSQKV + 64 * (4 * fp + kq));
    }
    st16k(rs, qf, lo, WS_Q + ((2 * tp + ti) * KS_D + 4 * fp + kq) * 1024);
  }
  if (jw < 56 && w8 == 7) {
    const LAS float* cp = (const LAS float*)(L + K1_S) + lane;
    const float s = (cp[0] + cp[2048]) + (cp[4096] + cp[6144]);
    __builtin_amdgcn_raw_buffer_store_b32(__float_as_uint(s), rs, lane * 4, WS_CS1 + (jw & 1) * 8192 + (jw >> 1) * 256, K1_AUX);
  }
  STAMP(0, 3);
}

DI lcp opaque(lcp p) { unsigned a = (unsigned)(uintptr_t)p; asm volatile("" : "+v"(a)); return (lcp)(uintptr_t)a; }
constexpr int M_A0 = 0, M_B0 = 32768, M_A1 = 65536, M_B1 = 98304;
constexpr int M_MS = 134 * 1024;
constexpr int M_TOP = 136 * 1024;
constexpr int MAIN_LDS = 160 * 1024;
static_assert(M_MS >= KIMG_BYTES + VIMG_BYTES + 2048, "K/V image must fit below the tables");
#define EX(x) __builtin_amdgcn_exp2f(x)

DI void attn_simple(rsrc_t rs, int kvb, const u32x4 qf, f32x16& o, int lane) {
  const int r = lane & 31, h = lane >> 5;
  float tm = -INFINITY;
  for (int t = 0; t < 64; ++t) {
    const u32x4 kx = ld16c(rs, lane * 16, kvb + t * 2048);
    f32x16 z = {};
    f32x16 s = MFMA(kx, qf, z);
#pragma unroll
    for (int i = 0; i < 16; ++i) tm = __builtin_fmaxf(tm, s[i]);
  }
  tm = xmax(tm);
  f32x16 negm;
#pragma unroll
  for (int i = 0; i < 16; ++i) { negm[i] = -tm; o[i] = 0.f; }
  const unsigned cr = r == 16 ? 0x3f803f80u : 0u;
  const u32x4 cv = {cr, cr, cr, cr};
  for (int t = 0; t < 64; ++t) {
    const u32x4 kf = ld16c(rs, lane * 16, kvb + t * 2048);
    u32x4 v0 = ld16c(rs, 1024 + h * 256 + (r & 15) * 16, kvb + t * 2048);
    u32x4 v1 = ld16c(rs, 1536 + h * 256 + (r & 15) * 16, kvb + t * 2048);
    if (r >= 16) { v0 = cv; v1 = cv; }
    f32x16 s = MFMA(kf, qf, negm);
#pragma unroll
    for (int i = 0; i < 16; ++i) s[i] = EX(s[i]);
    u32x4 p0 = {pk2(s[0], s[1]), pk2(s[2], s[3]), pk2(s[4], s[5]), pk2(s[6], s[7])};
    u32x4 p1 = {pk2(s[8], s[9]), pk2(s[10], s[11]), pk2(s[12], s[13]), pk2(s[14], s[15])};
    o = MFMA(v0, p0, o);
    o = MFMA(v1, p1, o);
  }
}

DI void put_tile(const f32x16& a, LAS char* p, int owner_stride) {
#pragma unroll
  for (int g = 0; g < 4; ++g) {
    f32x4 v = {a[4 * g], a[4 * g + 1], a[4 * g + 2], a[4 * g + 3]};
    *(LAS f32x4*)(p + (g >> 1) * owner_stride + (g & 1) * 1024) = v;
  }
}
template <int N> DI void get_half(float (&o)[8], lcp p) {
  f32x4 v[N][2];
#pragma unroll
  for (int s = 0; s < N; ++s) { v[s][0] = *(const LAS f32x4*)(p + s * 2048); v[s][1] = *(const LAS f32x4*)(p + s * 2048 + 1024); }
#pragma unroll
  for (int s = 0; s < N; ++s)
#pragma unroll
    for (int e = 0; e < 4; ++e) { o[e] += v[s][0][e]; o[4 + e] += v[s][1][e]; }
}
DI u32x4 publish_x(rsrc_t rs, const float (&o)[8], int xoff, int stoff, int lane) {
  const int r = lane & 31, h = lane >> 5;
  const u32x4 y = {pk2(o[0], o[1]), pk2(o[2], o[3]), pk2(o[4], o[5]), pk2(o[6], o[7])};
  st16c(rs, y, lane * 16, xoff);
  SBAR();
  float v[8];
#pragma unroll
  for (int i = 0; i < 4; ++i) { v[2 * i] = bflo(y[i]); v[2 * i + 1] = bfhi(y[i]); }
  float s = ((v[0] + v[1]) + (v[2] + v[3])) + ((v[4] + v[5]) + (v[6] + v[7]));
  s = xsum(s);
  const float mu = s * (1.0f / 16.0f);
  float m2 = 0.f;
#pragma unroll
  for (int i = 0; i < 8; ++i) { const float d = v[i] - mu; m2 = __builtin_fmaf(d, d, m2); }
  m2 = xsum(m2);
  const f32x2 st = {s, m2};
  st8c(rs, st, h ? 0x30000000 : r * 16, stoff);
  return y;
}
template <int CTRL> DI float dpp(float v) { return __int_as_float(__builtin_amdgcn_update_dpp(0, __float_as_int(v), CTRL, 0xf, 0xf, true)); }
DI float sum8(float v) { v += dpp<0xB1>(v); v += dpp<0x4E>(v); v += dpp<0x141>(v); return v; }
DI f32x2 combine4(const u32x4 a, const u32x4 b) {
  const float s0 = __uint_as_float(a[0]), s1 = __uint_as_float(a[2]), s2 = __uint_as_float(b[0]), s3 = __uint_as_float(b[2]);
  const float mean = sum8((s0 + s1) + (s2 + s3)) * (1.0f / 512.0f);
  const float d0 = __builtin_fmaf(s0, 1.0f / 16.0f, -mean), d1 = __builtin_fmaf(s1, 1.0f / 16.0f, -mean);
  const float d2 = __builtin_fmaf(s2, 1.0f / 16.0f, -mean), d3 = __builtin_fmaf(s3, 1.0f / 16.0f, -mean);
  const float dd = (d0 * d0 + d1 * d1) + (d2 * d2 + d3 * d3);
  const float m2 = (__uint_as_float(a[1]) + __uint_as_float(a[3])) + (__uint_as_float(b[1]) + __uint_as_float(b[3]));
  const float tot = sum8(__builtin_fmaf(dd, 16.0f, m2));
  f32x2 r = {mean, 1.0f / __builtin_sqrtf(tot * (1.0f / 512.0f) + 1e-5f)};
  return r;
}

constexpr int NW2P = 0;

__global__ void __launch_bounds__(512) main_kernel(char* ws, const unsigned short* xin, unsigned short* out) {
  extern __shared__ __attribute__((aligned(16))) char smem[];
  LAS char* L = (LAS char*)smem;
  const rsrc_t rs = ws_rsrc(ws);
  const unsigned lds0 = (unsigned)(uintptr_t)smem;
  const int w = __builtin_amdgcn_readfirstlane(threadIdx.x >> 6);
  const int xcd = blockIdx.x & 7, idx = blockIdx.x >> 3;
  const int tp = 4 * xcd + (idx >> 3), fp = idx & 7;
  const int kq = w >> 1, ti = w & 1;
  const int tt = 2 * tp + ti, og = 4 * fp + kq;
  int lane = threadIdx.x & 63, r = lane & 31, h = lane >> 5, lo = lane * 16;
#define RELANE() do { lane = __builtin_amdgcn_mbcnt_hi(~0u, __builtin_amdgcn_mbcnt_lo(~0u, 0u)); asm volatile("" : "+v"(lane)); lane &= 63; r = lane & 31; h = lane >> 5; lo = lane * 16; } while (0)
  const int own = (tt * KS_D + og) * 1024;
  const int ownst = tt * 8192 + (og >> 1) * 512 + (og & 1) * 8;
  const int ft1 = 7 * fp + (w < 7 ? w : 6);
  const int w1o = w < 7 ? WS_W1 + ft1 * KS_D * 1024 : 0x40000000;
  const int woo = WS_WO + ((2 * fp) * KS_D + 8 * w) * 1024;
  const int w2o = WS_W2 + ((2 * fp) * KS_F + 14 * w) * 1024;
  const int hown = w < 7 ? ((2 * tp) * KS_F + 2 * ft1) * 1024 : 0x20000000;
  const int stw = (2 * tp + (w >> 2)) * 8192 + (8 * (w & 3)) * 16;

  u32x4 qf = ld16(rs, lo, WS_Q + own);
  u32x4 res;
  {
    const unsigned short* xp = xin + (size_t)(32 * tt + r) * D_ + 16 * og + 4 * h;
    const u32x2 a0 = *(const u32x2*)xp, a1 = *(const u32x2*)(xp + 8);
    res = (u32x4){a0[0], a0[1], a1[0], a1[1]};
  }

#pragma unroll 1
  for (int layer = 0; layer < 2; ++layer) {
    const int hb = WS_HB + layer * HB_STR;
    const int KID = 2 + layer;
    STAMP(KID, 0);
    RELANE();
    u32x4 w1f[32], w2f[2][14];
#define W2LOAD(n) w2f[(n) & 1][(n) >> 1] = LDK(sop(w2o + ((n) & 1) * KS_F * 1024), (n) >> 1)
#define DMA(goff, loff) glds16(ws + (size_t)(unsigned)sop(goff) + lo, lds0 + (loff))
    {
      const int kva_ = sop(WS_KV + layer * KV_STR + 4 * w * 1024), kvb_ = sop(WS_KV + layer * KV_STR + (32 + 12 * w) * 1024);
      u32x4 ka[4], kb[12];
#define KVA(i) LDKC(kva_, i)
#define KVB(i) LDKC(kvb_, i)
#pragma unroll
      for (int i = 0; i < 4; ++i) ka[i] = KVA(i);
#pragma unroll
      for (int i = 0; i < 12; ++i) kb[i] = KVB(i);
      SBAR();
      if (h == 0) {
        const unsigned c = (lane & 1) ? 0u : 0x3f803f80u;
        const u32x4 cv = {c, c, c, c};
        *(LAS u32x4*)(L + KIMG_BYTES + (16 * w + (lane >> 1)) * VBLK + 512 + (lane & 1) * 16) = cv;
      }
      LAS char* kdst = L + lo;
      LAS char* vdst = L + KIMG_BYTES + h * VBLK + r * 16;
      {
        VERIFY(4, ka, KVA(i));
#pragma unroll
        for (int i = 0; i < 2; ++i) { *(LAS u32x4*)(kdst + (2 * w + i) * 1024) = ka[2 * i]; *(LAS u32x4*)(vdst + (2 * w + i) * 2 * VBLK) = ka[2 * i + 1]; }
      }
      BAR();
      STAMP(KID, 12);
      const lcp kp0 = (lcp)L + lo;
      const lcp vp0 = (lcp)L + KIMG_BYTES + (r < 16 ? h * 256 + r * 16 : (r > 16) ? 528 : 512);
      lcp kq_ = opaque(kp0), vq = opaque(vp0);
      f32x16 negm, o;
      u32x4 kf = *(lds_u4p)kq_;
      {
        f32x16 z = {};
        f32x16 s = MFMA(kf, qf, z);
        float m = s[0];
#pragma unroll
        for (int i = 1; i < 16; ++i) m = __builtin_fmaxf(m, s[i]);
        m = xmax(m);
#pragma unroll
        for (int i = 0; i < 16; ++i) { negm[i] = -m; o[i] = 0.f; }
        PIN(negm);
      }
      f32x16 sa = MFMA(kf, qf, negm), sb;
      kf = *(lds_u4p)(kq_ + 1024);
      u32x4 v0 = *(lds_u4p)vq;
      u32x4 p1 = {0, 0, 0, 0}, v1p = v0;
      unsigned sv1;
      asm volatile("v_mov_b32 %0, -1" : "=v"(sv1));
      const u32x4 sv4 = {sv1, sv1, sv1, sv1};
      const u32x2 sv2 = {sv4[0], sv4[1]};
      const int fz = layer == 0 ? 0 : 0x40000000;
#define FILL(off, k) do { __builtin_amdgcn_raw_buffer_store_b128(sv4, rs, lo + (k) * 1024, sop(fz + (off)), AUX_SC1); ST_PAD(sv4); } while (0)
#define FILLST(off) __builtin_amdgcn_raw_buffer_store_b64(sv2, rs, h ? 0x30000000 : r * 16, sop(fz + (off) + ownst), AUX_SC1)
      auto side = [&](auto NC) {
        constexpr int n = decltype(NC)::value;
        if constexpr (n == 0) FILL(WS_HB + HB_X1 + own, 0);
        else if constexpr (n < 5) { constexpr int j = n - 1; FILL(WS_HB + HB_H + hown + (j >> 1) * KS_F * 1024, j & 1); }
        else if constexpr (n == 5) FILL(WS_X2 + own, 0);
        else if constexpr (n == 6) FILL(WS_HB + HB_STR + HB_O + own, 0);
        else if constexpr (n == 7) FILL(WS_HB + HB_STR + HB_X1 + own, 0);
        else if constexpr (n < 12) { constexpr int j = n - 8; FILL(WS_HB + HB_STR + HB_H + hown + (j >> 1) * KS_F * 1024, j & 1); }
        else if constexpr (n == 12) FILLST(WS_HB + HB_ST1);
        else if constexpr (n == 13) FILLST(WS_ST2);
        else if constexpr (n == 14) FILLST(WS_HB + HB_STR + HB_ST1);
        else if constexpr (n == 15) { }
        else if constexpr (n < 48) w1f[n - 16] = LDK(sop(w1o), n - 16);
        else if constexpr (n < 56) { constexpr int j = n - 48; DMA(woo + j * 1024, (w >> 2) * M_A1 + ((w & 3) * 8 + j) * 1024); }
        else if constexpr (n < 59) { constexpr int j = 25 + n - 56; DMA(w2o + (j & 1) * KS_F * 1024 + (j >> 1) * 1024, M_TOP + (w * 3 + n - 56) * 1024); }
        else if constexpr (n < 60) { }
        else if constexpr (n < 60 + NW2P) W2LOAD(n - 60);
      };
#define SIDE(n) do { if constexpr ((n) >= 0) side(std::integral_constant<int, (n) < 0 ? 0 : (n)>{}); } while (0)
#define STEP2(sc, sn, koff, voff, N) do { u32x4 p0; \
    sc[0] = EX(sc[0]); sc[1] = EX(sc[1]); sc[2] = EX(sc[2]); sc[3] = EX(sc[3]); sc[4] = EX(sc[4]); SBAR(); \
    sn = MFMA(kf, qf, negm); SBAR(); \
    kf = *(lds_u4p)(kq_ + (koff)); SIDE(N); SBAR(); \
    sc[5] = EX(sc[5]); sc[6] = EX(sc[6]); sc[7] = EX(sc[7]); sc[8] = EX(sc[8]); sc[9] = EX(sc[9]); SBAR(); \
    o = MFMA(v1p, p1, o); SBAR(); \
    v1p = *(lds_u4p)(vq + (voff) - VBLK); SBAR(); \
    p0[0] = pk2(sc[0], sc[1]); p0[1] = pk2(sc[2], sc[3]); p0[2] = pk2(sc[4], sc[5]); p0[3] = pk2(sc[6], sc[7]); SBAR(); \
    sc[10] = EX(sc[10]); sc[11] = EX(sc[11]); sc[12] = EX(sc[12]); sc[13] = EX(sc[13]); sc[14] = EX(sc[14]); sc[15] = EX(sc[15]); SBAR(); \
    o = MFMA(v0, p0, o); SBAR(); \
    v0 = *(lds_u4p)(vq + (voff)); SIDE((N) < 0 ? -1 : (N) + 1); SBAR(); \
    p1[0] = pk2(sc[8], sc[9]); p1[1] = pk2(sc[10], sc[11]); p1[2] = pk2(sc[12], sc[13]); p1[3] = pk2(sc[14], sc[15]); SBAR(); } while (0)
#define STEP4(N) do { \
      STEP2(sa, sb, 2 * 1024, 2 * VBLK, N); \
      STEP2(sb, sa, 3 * 1024, 4 * VBLK, (N) < 0 ? -1 : (N) + 2); \
      STEP2(sa, sb, 4 * 1024, 6 * VBLK, (N) < 0 ? -1 : (N) + 4); \
      STEP2(sb, sa, 5 * 1024, 8 * VBLK, (N) < 0 ? -1 : (N) + 6); \
      kq_ += 4096; vq += 8 * VBLK; } while (0)
#pragma unroll 1
      for (int t = 0; t < 8; t += 4) STEP4(-1);
      {
        VERIFY(12, kb, KVB(i));
#pragma unroll
        for (int i = 0; i < 6; ++i) { *(LAS u32x4*)(kdst + (16 + 6 * w + i) * 1024) = kb[2 * i]; *(LAS u32x4*)(vdst + (16 + 6 * w + i) * 2 * VBLK) = kb[2 * i + 1]; }
      }
      BAR();
      STAMP(KID, 13);
      STEP4(0); STEP4(8); STEP4(16); STEP4(24); STEP4(32); STEP4(40);
      STEP4(-1);
      BAR();
      STEP4(48); STEP4(56);
#pragma unroll 1
      for (int t = 44; t < 64; t += 4) STEP4(-1);
      o = MFMA(v1p, p1, o);
      STAMP(KID, 14);
      float l = lowhalf(o[8]);
      bool bad = !(l < 1.0e18f);
#pragma unroll
      for (int i = 0; i < 8; ++i) bad |= !(__builtin_fabsf(o[i]) < 3.0e38f);
      if (__builtin_expect(__any(bad), 0)) {
        attn_simple(rs, WS_KV + layer * KV_STR, qf, o, lane);
        l = lowhalf(o[8]);
      }
      const float rl = 1.0f / l;
      const u32x4 oa = {pk2(o[0] * rl, o[1] * rl), pk2(o[2] * rl, o[3] * rl), pk2(o[4] * rl, o[5] * rl), pk2(o[6] * rl, o[7] * rl)};
      asm volatile("s_waitcnt vmcnt(0)" ::: "memory");
      st16c(rs, oa, lo, sop(hb + HB_O + own));
      BAR();
    }
    STAMP(KID, 15);
    RELANE();
    {
      u32x4 af[8], wo0[8], wo1[8];
      const lcp wp = (lcp)L + 8 * kq * 1024 + lo;
#pragma unroll
      for (int i = 0; i < 8; ++i) { wo0[i] = *(lds_u4p)(wp + M_A0 + i * 1024); PIN(wo0[i]); }
#pragma unroll
      for (int i = 0; i < 2; ++i) { wo1[i] = *(lds_u4p)(opaque(wp + M_A1) + i * 1024); PIN(wo1[i]); }
      const int aa_ = sop(hb + HB_O + (tt * KS_D + 8 * kq) * 1024);
      HANDOFF_LOAD(8, af, LDKC(aa_, i), fold4(m_, af[i]));
      STAMP(KID, 1);
      f32x16 a0 = {}, a1 = {};
      {
        const lcp wp1 = opaque(wp + M_A1);
#pragma unroll
        for (int i = 0; i < 8; ++i) {
          a0 = MFMA(wo0[i], af[i], a0);
          if (i < 6) wo1[2 + i] = *(lds_u4p)(wp1 + (2 + i) * 1024);
          SBAR();
        }
#pragma unroll
        for (int i = 0; i < 8; ++i) {
          a1 = MFMA(wo1[i], af[i], a1);
          if (i == 1) put_tile(a0, L + M_B0 + (ti * 4 + kq) * 2048 + lo, 2 * 8192);
          SBAR();
        }
        put_tile(a1, L + M_B1 + (ti * 4 + kq) * 2048 + lo, 2 * 8192);
      }
      BAR();
      float o[8];
#pragma unroll
      for (int i = 0; i < 4; ++i) { o[2 * i] = bflo(res[i]); o[2 * i + 1] = bfhi(res[i]); }
      get_half<4>(o, (lcp)L + M_B0 + (w >> 2) * 65536 + (w & 3) * 8192 + lo);
      (void)publish_x(rs, o, sop(hb + HB_X1 + own), sop(hb + HB_ST1 + ownst), lane);
      SBAR();
    }
    STAMP(KID, 2);
    RELANE();
    f32x4 cs[4], c2[4];
#pragma unroll
    for (int g = 0; g < 4; ++g) { cs[g] = __builtin_bit_cast(f32x4, ld16(rs, h * 16 + g * 32, sop(WS_CS1 + 128 * ft1))); c2[g] = __builtin_bit_cast(f32x4, ld16(rs, h * 16 + g * 32, sop(WS_CS1 + 8192 + 128 * ft1))); }
#pragma unroll
    for (int n = 0; n < 4; ++n) W2LOAD(n);
    SBAR();
    STAMP(KID, 3);
    {
      u32x4 xf[8];
      const int xo_ = sop(hb + HB_X1 + ((2 * tp) * KS_D + 8 * w) * 1024);
      HANDOFF_LOAD(8, xf, LDKC(xo_, i), fold4(m_, xf[i]));
#pragma unroll
      for (int i = 0; i < 8; ++i) *(LAS u32x4*)(L + (w >> 2) * M_A1 + (8 * (w & 3) + i) * 1024 + lo) = xf[i];
#pragma unroll
      for (int g = 0; g < 4; ++g) { cs[g] += c2[g]; PIN(cs[g]); }
      BAR();
    }
    STAMP(KID, 4);
    u32x4 h0[14], h1[14];
    const int h0_ = hb + HB_H + ((2 * tp) * KS_F + 14 * w) * 1024;
#define H0LOAD(i) LDKC(sop(h0_), i)
#define H1LOAD(i) LDKC(sop(h0_ + KS_F * 1024), i)
    {
      const lcp xp = (lcp)L + lo;
#define XF(i) (*(lds_u4p)(xp + ((i) >> 5) * M_A1 + ((i) & 31) * 1024))
      auto epi = [&](const f32x16& o, f32x2 ms, int hoff) {
        const float nm = -ms[0], sc = ms[1];
        u32x4 e0, e1;
#pragma unroll
        for (int g = 0; g < 4; ++g) {
          const float a = __builtin_fmaxf(__builtin_fmaf(nm, cs[g][0], o[4 * g]) * sc, 0.f), b = __builtin_fmaxf(__builtin_fmaf(nm, cs[g][1], o[4 * g + 1]) * sc, 0.f);
          const float c = __builtin_fmaxf(__builtin_fmaf(nm, cs[g][2], o[4 * g + 2]) * sc, 0.f), d = __builtin_fmaxf(__builtin_fmaf(nm, cs[g][3], o[4 * g + 3]) * sc, 0.f);
          if (g < 2) { e0[2 * g] = pk2(a, b); e0[2 * g + 1] = pk2(c, d); }
          else { e1[2 * (g - 2)] = pk2(a, b); e1[2 * (g - 2) + 1] = pk2(c, d); }
        }
        const int eo_ = sop(hb + HB_H + hoff);
        st16c(rs, e0, lo, eo_); st16c(rs, e1, lo + 1024, eo_);
      };
      f32x16 a0 = {};
      u32x4 st[2];
      const int so_ = sop(hb + HB_ST1 + stw);
#define STLOAD(i) ld16c(rs, (lane & 7) * 1024 + (lane >> 3) * 16 + (i) * 512, so_)
      st[0] = STLOAD(0); st[1] = STLOAD(1);
      constexpr int PF = 4;
      u32x4 xa[PF];
#pragma unroll
      for (int i = 0; i < PF; ++i) xa[i] = XF(i);
#pragma unroll
      for (int i = 0; i < 32; ++i) {
        a0 = MFMA(w1f[i], xa[i % PF], a0);
        xa[i % PF] = XF(i + PF);
        if ((i & 1) == 0 && i < 16) DMA(w2o + ((i >> 1) & 1) * KS_F * 1024 + (4 + (i >> 2)) * 1024, M_B0 + (w >> 2) * 65536 + ((w & 3) * 8 + (i >> 1)) * 1024);
        SBAR();
      }
      PIN(a0);
      VERIFY(2, st, STLOAD(i));
      {
        const f32x2 ms = combine4(st[0], st[1]);
        if ((lane & 7) == 0) *(LAS f32x2*)(L + M_MS + (8 * w + (lane >> 3)) * 8) = ms;
      }
      asm volatile("s_waitcnt vmcnt(0)" ::: "memory");
      BAR();
      STAMP(KID, 11);
      const f32x2 ms0 = *(const LAS f32x2*)(L + M_MS + r * 8), ms1 = *(const LAS f32x2*)(L + M_MS + (32 + r) * 8);
      epi(a0, ms0, hown);
      SBAR();
      f32x16 a1 = {};
#pragma unroll
      for (int i = 0; i < 32; ++i) {
        a1 = MFMA(w1f[i], xa[i % PF], a1);
        if (i + PF < 32) xa[i % PF] = XF(32 + i + PF);
        if (i >= 1 && i < 5) W2LOAD(3 + i);
        if (i >= 5 && i < 14) W2LOAD(11 + i);
        if (i >= 18) h0[i - 18] = H0LOAD(i - 18);
        SBAR();
      }
      PIN(a1);
      epi(a1, ms1, hown + KS_F * 1024);
      SBAR();
    }
    STAMP(KID, 5);
    RELANE();
    u32x4 wq[2][8], wkv[8];
    float o[8];
    {
      const int wqo = WS_WQKV + ((2 * fp) * KS_D + 8 * kq) * 1024;
      LAS char* rp = L + w * 2048 + lo;
#pragma unroll
      for (int n = 8; n < 16; ++n) w2f[n & 1][n >> 1] = *(lds_u4p)((lcp)L + M_B0 + (w >> 2) * 65536 + ((w & 3) * 8 + n - 8) * 1024 + lo);
      w2f[1][12] = *(lds_u4p)((lcp)L + M_TOP + (w * 3) * 1024 + lo);
      w2f[0][13] = *(lds_u4p)((lcp)L + M_TOP + (w * 3 + 1) * 1024 + lo);
      w2f[1][13] = *(lds_u4p)((lcp)L + M_TOP + (w * 3 + 2) * 1024 + lo);
      const u32x4 y1 = *(lds_u4p)((lcp)L + ti * M_A1 + og * 1024 + lo);
      BAR();
      {
        VERIFY(14, h0, H0LOAD(i));
      }
      f32x16 a0 = {}, a1 = {};
#pragma unroll
      for (int i = 0; i < 14; ++i) {
        a0 = MFMA(w2f[0][i], h0[i], a0);
        a1 = MFMA(w2f[1][i], h0[i], a1);
        if (i >= 4) { h1[i - 4] = H1LOAD(i - 4); if (i >= 10) h1[i] = H1LOAD(i); }
        SBAR();
      }
      put_tile(a0, rp + M_B0, 16384);
      put_tile(a1, rp + M_B1, 16384);
      STAMP(KID, 6);
      {
        VERIFY(14, h1, H1LOAD(i));
      }
      f32x16 b0 = {}, b1 = {};
#pragma unroll
      for (int i = 0; i < 14; ++i) {
        b0 = MFMA(w2f[0][i], h1[i], b0);
        if (i < 8) wq[0][i] = LDK(sop(wqo), i);
        SBAR();
      }
      put_tile(b0, rp + M_A0, 16384);
#pragma unroll
      for (int i = 0; i < 14; ++i) {
        b1 = MFMA(w2f[1][i], h1[i], b1);
        if (i < 8) wq[1][i] = LDK(sop(wqo + KS_D * 1024), i);
        SBAR();
      }
      put_tile(b1, rp + M_A1, 16384);
      STAMP(KID, 7);
      BAR();
      RELANE();
#pragma unroll
      for (int i = 0; i < 4; ++i) { o[2 * i] = bflo(y1[i]); o[2 * i + 1] = bfhi(y1[i]); }
      get_half<8>(o, (lcp)L + (ti ? M_A0 : M_B0) + (kq >> 1) * 65536 + (kq & 1) * 16384 + lo);
    }
    if (layer == 1) {
      const u32x2 a0 = {pk2(o[0], o[1]), pk2(o[2], o[3])}, a1 = {pk2(o[4], o[5]), pk2(o[6], o[7])};
      unsigned short* yp = out + (size_t)(32 * tt + r) * D_ + 16 * og + 4 * h;
      *(u32x2*)(yp) = a0; *(u32x2*)(yp + 8) = a1;
      STAMP(KID, 8);
      return;
    }
    res = publish_x(rs, o, sop(WS_X2 + own), sop(WS_ST2 + ownst), lane);
    SBAR();
    const bool kvw = fp < 2 && ti == fp;
    f32x4 cq[2], csk[2];
#pragma unroll
    for (int i = 0; i < 8; ++i) wkv[i] = LDK(sop(kvw ? WS_WQKV + (16 * KS_D + 8 * kq) * 1024 : 0x40000000), i);
#pragma unroll
    for (int g = 0; g < 2; ++g) cq[g] = __builtin_bit_cast(f32x4, ld16(rs, h * 16 + g * 32, sop(WS_CSQKV + 64 * og)));
#pragma unroll
    for (int g = 0; g < 2; ++g) csk[g] = __builtin_bit_cast(f32x4, ld16(rs, h * 16 + g * 32, WS_CSQKV + 2048));
    const float csv = __builtin_bit_cast(float, __builtin_amdgcn_raw_buffer_load_b32(rs, r * 4, WS_CSQKV + 2048, 0));
    SBAR();
    STAMP(KID, 8);
    {
      u32x4 xf[8], st[2];
      const int xa_ = sop(WS_X2 + (tt * KS_D + 8 * kq) * 1024), so_ = sop(WS_ST2 + stw);
      int spins = 0;
      for (;;) {
        asm volatile("" ::: "memory");
#pragma unroll
        for (int i = 0; i < 8; ++i) xf[i] = LDKC(xa_, i);
        st[0] = STLOAD(0); st[1] = STLOAD(1);
        unsigned m = fold4(fold4(0u, st[0]), st[1]);
#pragma unroll
        for (int i = 0; i < 8; ++i) m = fold4(m, xf[i]);
        if (!__any(m == SENT) || ++spins > SPIN_CAP) break;
      }
      STAMP(KID, 9);
      f32x16 aK = {}, aV = {};
      if (kvw) {
#pragma unroll
        for (int i = 0; i < 8; ++i) { aV = MFMA(xf[i], wkv[i], aV); aK = MFMA(wkv[i], xf[i], aK); }
      }
      f32x16 a0 = {}, a1 = {};
#pragma unroll
      for (int i = 0; i < 8; ++i) { a0 = MFMA(wq[0][i], xf[i], a0); a1 = MFMA(wq[1][i], xf[i], a1); }
      {
        const f32x2 msw = combine4(st[0], st[1]);
        if ((lane & 7) == 0) *(LAS f32x2*)(L + M_MS + (8 * w + (lane >> 3)) * 8) = msw;
      }
      BAR();
      if (kvw) {
        LAS char* kp = L + M_TOP + kq * 6144 + lo;
#pragma unroll
        for (int g = 0; g < 4; ++g) { f32x4 u = {aV[4 * g], aV[4 * g + 1], aV[4 * g + 2], aV[4 * g + 3]}; *(LAS f32x4*)(kp + g * 1024) = u; }
#pragma unroll
        for (int g = 0; g < 2; ++g) { f32x4 u = {aK[4 * g], aK[4 * g + 1], aK[4 * g + 2], aK[4 * g + 3]}; *(LAS f32x4*)(kp + (4 + g) * 1024) = u; }
      }
      put_tile(a0, L + M_B0 + (ti * 4 + kq) * 2048 + lo, 2 * 8192);
      put_tile(a1, L + M_B1 + (ti * 4 + kq) * 2048 + lo, 2 * 8192);
      BAR();
      if (fp < 2 && ti != fp && kq < 3) {
        const int kvo = sop(WS_KV + KV_STR + (2 * tp + fp) * 2048);
        const lcp kp = (lcp)L + M_TOP + lo;
        if (kq == 2) {
          const f32x2 mk = *(const LAS f32x2*)(L + M_MS + (32 * fp + r) * 8);
          const float mu = mk[0], rsd = mk[1];
          float k[8];
#pragma unroll
          for (int g = 0; g < 2; ++g) {
            f32x4 s = {0.f, 0.f, 0.f, 0.f};
#pragma unroll
            for (int sw = 0; sw < 4; ++sw) s += *(const LAS f32x4*)(kp + sw * 6144 + (4 + g) * 1024);
#pragma unroll
            for (int e = 0; e < 4; ++e) k[4 * g + e] = __builtin_fmaf(-mu, csk[g][e], s[e]) * rsd;
          }
          const u32x4 ko = {pk2(k[0], k[1]), pk2(k[2], k[3]), pk2(k[4], k[5]), pk2(k[6], k[7])};
          st16c(rs, ko, lo, kvo);
        } else {
          float v[8];
#pragma unroll
          for (int g2 = 0; g2 < 2; ++g2) {
            f32x4 s = {0.f, 0.f, 0.f, 0.f};
#pragma unroll
            for (int sw = 0; sw < 4; ++sw) s += *(const LAS f32x4*)(kp + sw * 6144 + (2 * kq + g2) * 1024);
            const lcp mp = (lcp)L + M_MS + (32 * fp + 16 * kq + 8 * g2 + 4 * h) * 8;
            const f32x4 m01 = *(const LAS f32x4*)mp, m23 = *(const LAS f32x4*)(mp + 16);
            v[4 * g2] = __builtin_fmaf(-m01[0], csv, s[0]) * m01[1]; v[4 * g2 + 1] = __builtin_fmaf(-m01[2], csv, s[1]) * m01[3];
            v[4 * g2 + 2] = __builtin_fmaf(-m23[0], csv, s[2]) * m23[1]; v[4 * g2 + 3] = __builtin_fmaf(-m23[2], csv, s[3]) * m23[3];
          }
          const u32x4 vo = {pk2(v[0], v[1]), pk2(v[2], v[3]), pk2(v[4], v[5]), pk2(v[6], v[7])};
          st16c(rs, vo, r >= 16 ? 1024 + kq * 512 + h * 256 + (r - 16) * 16 : 0x30000000, kvo);
        }
      }
      float q[8] = {0.f, 0.f, 0.f, 0.f, 0.f, 0.f, 0.f, 0.f};
      get_half<4>(q, (lcp)L + M_B0 + (w >> 2) * 65536 + (w & 3) * 8192 + lo);
      const f32x2 ms = *(const LAS f32x2*)(L + M_MS + (32 * ti + r) * 8);
      const float nm = -ms[0], sc = ms[1] * QSCALE;
#pragma unroll
      for (int g = 0; g < 2; ++g) {
        qf[2 * g] = pk2(__builtin_fmaf(nm, cq[g][0], q[4 * g]) * sc, __builtin_fmaf(nm, cq[g][1], q[4 * g + 1]) * sc);
        qf[2 * g + 1] = pk2(__builtin_fmaf(nm, cq[g][2], q[4 * g + 2]) * sc, __builtin_fmaf(nm, cq[g][3], q[4 * g + 3]) * sc);
      }
      STAMP(KID, 10);
      BAR();
    }
  }
}

extern "C" void kernel_launch(void* const* d_in, const int* in_sizes, int n_in, void* d_out, int out_size, void* d_ws, size_t ws_size,
                              hipStream_t stream) {
  const unsigned short* x = (const unsigned short*)d_in[0];
  const unsigned short* wq = (const unsigned short*)d_in[1];
  const unsigned short* wk = (const unsigned short*)d_in[2];
  const unsigned short* wv = (const unsigned short*)d_in[3];
  const unsigned short* wo = (const unsigned short*)d_in[4];
  const unsigned short* w1 = (const unsigned short*)d_in[6];
  const unsigned short* w2 = (const unsigned short*)d_in[7];
  char* ws = (char*)d_ws;
  (void)hipFuncSetAttribute((const void*)first_kernel, hipFuncAttributeMaxDynamicSharedMemorySize, K1_LDS);
  (void)hipFuncSetAttribute((const void*)main_kernel, hipFuncAttributeMaxDynamicSharedMemorySize, MAIN_LDS);
  first_kernel<<<256, 512, K1_LDS, stream>>>(x, wq, wk, wv, wo, w1, w2, ws);
  main_kernel<<<256, 512, MAIN_LDS, stream>>>(ws, x, (unsigned short*)d_out);
}
```

```cpp
#include <hip/hip_runtime.h>
#include <hip/hip_bf16.h>
#include <stdint.h>
#include <type_traits>

typedef __attribute__((ext_vector_type(8))) __bf16 bf16x8;
typedef __attribute__((ext_vector_type(2))) __bf16 bf16x2;
typedef __attribute__((ext_vector_type(16))) float f32x16;
typedef __attribute__((ext_vector_type(4))) float f32x4;
typedef __attribute__((ext_vector_type(2))) float f32x2;
typedef __attribute__((ext_vector_type(4))) unsigned u32x4;
typedef __attribute__((ext_vector_type(2))) unsigned u32x2;
#define DI __device__ __forceinline__
#define MFMA(a, b, c) __builtin_amdgcn_mfma_f32_32x32x16_bf16(__builtin_bit_cast(bf16x8, (a)), __builtin_bit_cast(bf16x8, (b)), (c), 0, 0, 0)
#define SBAR() __builtin_amdgcn_sched_barrier(0)
#define PIN(x) asm volatile("" : "+v"(x))
#define PINA(x) asm volatile("" : "+a"(x))
#define LAS __attribute__((address_space(3)))
#define GAS __attribute__((address_space(1)))

constexpr int S_ = 2048, D_ = 512, DFF = 1792;
constexpr int NTT = S_ / 32;
constexpr int KS_D = D_ / 16;
constexpr int KS_F = DFF / 16;
constexpr int VBLK = 544;
constexpr int KIMG_BYTES = NTT * 1024;
constexpr int VIMG_BYTES = NTT * 2 * VBLK;
constexpr float QSCALE = 0.25f * 1.4426950408889634f;

constexpr int MB2 = 2 * 1024 * 1024;
constexpr int WS_WQKV = 0;
constexpr int WS_WO   = WS_WQKV + 17 * 32 * 1024;
constexpr int WS_W1   = WS_WO + 16 * 32 * 1024;
constexpr int WS_W2   = WS_W1 + 56 * 32 * 1024;
constexpr int WS_CSQKV = WS_W2 + 16 * 112 * 1024;
constexpr int WS_CS1  = WS_CSQKV + 4096;
constexpr int WS_Q    = WS_CS1 + 16384;
constexpr int WS_KV   = WS_Q + MB2;
constexpr int KV_STR  = 128 * 1024;
constexpr int HB_O = 0, HB_X1 = MB2, HB_ST1 = 2 * MB2, HB_H = HB_ST1 + 512 * 1024, HB_STR = HB_H + S_ * DFF * 2;
constexpr int WS_HB   = WS_KV + 2 * KV_STR;
constexpr int WS_X2   = WS_HB + 2 * HB_STR;
constexpr int WS_ST2  = WS_X2 + MB2;
constexpr int WS_END  = WS_ST2 + 512 * 1024;

#define STAMP(k, i)

DI unsigned pk2(float a, float b) { f32x2 v = {a, b}; bf16x2 r = __builtin_convertvector(v, bf16x2); return __builtin_bit_cast(unsigned, r); }
DI float bflo(unsigned u) { return __uint_as_float(u << 16); }
DI float bfhi(unsigned u) { return __uint_as_float(u & 0xffff0000u); }
DI float xsum(float v) {
  auto rr = __builtin_amdgcn_permlane32_swap(__float_as_uint(v), __float_as_uint(v), false, false);
  return __uint_as_float(rr[0]) + __uint_as_float(rr[1]);
}
DI float xmax(float v) {
  auto rr = __builtin_amdgcn_permlane32_swap(__float_as_uint(v), __float_as_uint(v), false, false);
  return __builtin_fmaxf(__uint_as_float(rr[0]), __uint_as_float(rr[1]));
}
DI float lowhalf(float v) {
  auto rr = __builtin_amdgcn_permlane32_swap(__float_as_uint(v), __float_as_uint(v), false, false);
  return __uint_as_float(rr[0]);
}
DI int perm16(int h, int j) { return 8 * (j >> 2) + 4 * h + (j & 3); }

typedef __amdgpu_buffer_rsrc_t rsrc_t;
constexpr int AUX_SC1 = 16;
DI rsrc_t ws_rsrc(char* ws) { return __builtin_amdgcn_make_buffer_rsrc((void*)ws, 0, WS_END, 0x00020000); }
DI u32x4 ld16(rsrc_t rs, int voff, int soff) { return __builtin_amdgcn_raw_buffer_load_b128(rs, voff, soff, 0); }
DI u32x4 ld16c(rsrc_t rs, int voff, int soff) { return __builtin_amdgcn_raw_buffer_load_b128(rs, voff, soff, AUX_SC1); }
DI u32x2 ld8c(rsrc_t rs, int voff, int soff) { return __builtin_amdgcn_raw_buffer_load_b64(rs, voff, soff, AUX_SC1); }
#define ST_PAD(v) asm volatile("s_nop 1" :: "v"(v) : "memory")
DI void st16(rsrc_t rs, u32x4 v, int voff, int soff) { __builtin_amdgcn_raw_buffer_store_b128(v, rs, voff, soff, 0); ST_PAD(v); }
DI int sop(int x) { asm volatile("" : "+s"(x)); return x; }
#define LDK(base, k) ld16(rs, lo + ((k) & 3) * 1024, (base) + ((k) >> 2) * 4096)
#define LDKC(base, k) ld16c(rs, lo + ((k) & 3) * 1024, (base) + ((k) >> 2) * 4096)
constexpr unsigned SENT = 0xffffffffu;
DI unsigned clean(unsigned u) { return u < SENT ? u : SENT - 1u; }
DI void st16c(rsrc_t rs, u32x4 v, int voff, int soff) {
  u32x4 c = {clean(v[0]), clean(v[1]), clean(v[2]), clean(v[3])};
  __builtin_amdgcn_raw_buffer_store_b128(c, rs, voff, soff, AUX_SC1);
  ST_PAD(c);
}
DI void st8c(rsrc_t rs, f32x2 v, int voff, int soff) {
  u32x2 c = {clean(__float_as_uint(v[0])), clean(__float_as_uint(v[1]))};
  __builtin_amdgcn_raw_buffer_store_b64(c, rs, voff, soff, AUX_SC1);
}
DI unsigned umax3(unsigned a, unsigned b, unsigned c) { unsigned m = a > b ? a : b; return m > c ? m : c; }
DI unsigned fold4(unsigned m, u32x4 v) { return umax3(umax3(m, v[0], v[1]), v[2], v[3]); }
constexpr int SPIN_CAP = 1 << 15;

#define HANDOFF_LOAD(N, DST, LOADEXPR, FOLD) do { \
    int spins_ = 0; \
    for (;;) { \
      asm volatile("" ::: "memory"); \
      _Pragma("unroll") for (int i = 0; i < (N); ++i) DST[i] = LOADEXPR; \
      unsigned m_ = 0; \
      _Pragma("unroll") for (int i = 0; i < (N); ++i) m_ = FOLD; \
      if (!__any(m_ == SENT) || ++spins_ > SPIN_CAP) break; \
      __builtin_amdgcn_s_sleep(2); \
    } } while (0)

#define VERIFY(N, DST, LOADEXPR) do { \
    int spins_ = 0; \
    _Pragma("nounroll") for (;;) { \
      unsigned m_ = 0; \
      _Pragma("unroll") for (int i = 0; i < (N); ++i) m_ = fold4(m_, DST[i]); \
      if (!__any(m_ == SENT) || ++spins_ > SPIN_CAP) break; \
      __builtin_amdgcn_s_sleep(1); \
      asm volatile("" ::: "memory"); \
      _Pragma("unroll") for (int i = 0; i < (N); ++i) DST[i] = LOADEXPR; \
    } } while (0)

#define BAR() do { asm volatile("s_waitcnt lgkmcnt(0)" ::: "memory"); __builtin_amdgcn_s_barrier(); asm volatile("" ::: "memory"); } while (0)

typedef LAS const u32x4* lds_u4p;
typedef const LAS char* lcp;
typedef short s16x4 __attribute__((ext_vector_type(4)));
DI void glds16(const void* g, unsigned lds_base) {
  unsigned sv;
  asm volatile("s_mov_b32 %0, m0\n\ts_mov_b32 m0, %2\n\ts_nop 0\n\tglobal_load_lds_dwordx4 %1, off\n\ts_mov_b32 m0, %0"
               : "=&s"(sv) : "v"(g), "s"(lds_base) : "memory");
}
DI u32x2 ldtr(lcp p) { return __builtin_bit_cast(u32x2, __builtin_amdgcn_ds_read_tr16_b64_v4i16((LAS s16x4*)p)); }
DI u32x4 tr_frag(lcp p, int rowbytes, int s) {
  const u32x2 a = ldtr(p + 16 * s * rowbytes), b = ldtr(p + (16 * s + 8) * rowbytes);
  u32x4 o = {a[0], a[1], b[0], b[1]};
  return o;
}
constexpr int FIRST_CODE_BYTES = 11264, MAIN_CODE_BYTES = 25600;
constexpr int K1_X = 0, K1_W = 65536, K1_S = 131072, K1_LDS = 163840;
#define K1_AUX 16
DI void st16k(rsrc_t rs, u32x4 v, int voff, int soff) { __builtin_amdgcn_raw_buffer_store_b128(v, rs, voff, soff, K1_AUX); ST_PAD(v); }
DI float dot2(unsigned a, unsigned b, float c) { return __builtin_amdgcn_fdot2_f32_bf16(__builtin_bit_cast(bf16x2, a), __builtin_bit_cast(bf16x2, b), c, false); }
DI void put4(const f32x16& a, LAS char* p, int n) {
#pragma unroll
  for (int g = 0; g < 4; ++g) if (g < n) { f32x4 v = {a[4 * g], a[4 * g + 1], a[4 * g + 2], a[4 * g + 3]}; *(LAS f32x4*)(p + g * 1024) = v; }
}

#define CODE_ADDR(sym, lo32, hi32) asm volatile("s_getpc_b64 s[40:41]\n\ts_add_u32 s40, s40, " #sym "@rel32@lo+4\n\ts_addc_u32 s41, s41, " #sym "@rel32@hi+12\n\ts_mov_b32 %0, s40\n\ts_mov_b32 %1, s41" \
    : "=s"(lo32), "=s"(hi32) :: "s40", "s41", "scc")
#define CODE_WARM 1

extern "C" __global__ void __launch_bounds__(512) first_kernel(const unsigned short* x, const unsigned short* wq, const unsigned short* wk,
                                                    const unsigned short* wv, const unsigned short* wo, const unsigned short* w1,
                                                    const unsigned short* w2, char* ws) {
  extern __shared__ __attribute__((aligned(16))) char smem[];
  LAS char* L = (LAS char*)smem;
  const unsigned lds0 = (unsigned)(uintptr_t)smem;
  const rsrc_t rs = ws_rsrc(ws);
  const int lane = threadIdx.x & 63, r = lane & 31, h = lane >> 5, lo = lane * 16;
  const int w8 = __builtin_amdgcn_readfirstlane(threadIdx.x >> 6), w = w8 & 3;
  const int xcd = blockIdx.x & 7, idx = blockIdx.x >> 3;
  const int tp = 4 * xcd + (idx >> 3), fp = idx & 7;
  const bool kv = fp < 2;
  const int trq = (lane & 15) >> 2, trp = lane & 3, trcb = (lane >> 4) & 1;
  const u32x4 ones = {0x3f803f80u, 0x3f803f80u, 0x3f803f80u, 0x3f803f80u};
  const int jw = kv ? 192 : (fp - 2) * 32 + tp;
  STAMP(0, 0);
  if (w8 < 4) {
    {
      const unsigned short* wsrc = wq + (size_t)(128 * w + (lane >> 3)) * 512 + 64 * fp + (lane & 7) * 8;
      const int xr = lane >> 4;
      if (kv) {
#pragma unroll
        for (int i = 0; i < 4; ++i) {
          glds16(wk + (size_t)128 * w * 16 + i * 512 + lane * 8, lds0 + K1_S + w * 8192 + i * 1024);
          glds16(wv + (size_t)128 * w * 16 + i * 512 + lane * 8, lds0 + K1_S + w * 8192 + 4096 + i * 1024);
        }
      }
#pragma unroll
      for (int i = 0; i < 16; ++i) {
        const int row = 4 * i + xr;
        glds16(x + (size_t)(64 * tp + row) * 512 + 128 * w + (((lane & 15) ^ (row & 15)) * 8), lds0 + K1_X + w * 16384 + i * 1024);
      }
#pragma unroll
      for (int i = 0; i < 16; ++i) glds16(wsrc + (size_t)i * 8 * 512, lds0 + K1_W + w * 16384 + i * 1024);
    }
    asm volatile("s_waitcnt vmcnt(16)" ::: "memory");
    u32x4 wf[2][8], xf[2][8];
    float s1[2] = {0.f, 0.f}, s2[2] = {0.f, 0.f};
#pragma unroll
    for (int b = 0; b < 2; ++b) {
      const lcp xp = (lcp)L + K1_X + w * 16384 + (32 * b + r) * 256 + h * 8;
#pragma unroll
      for (int s = 0; s < 8; ++s) {
        const u32x2 a = *(const LAS u32x2*)(xp + (((2 * s) ^ (r & 15)) * 16)), c = *(const LAS u32x2*)(xp + (((2 * s + 1) ^ (r & 15)) * 16));
        xf[b][s] = (u32x4){a[0], a[1], c[0], c[1]};
      }
    }
    f32x16 aV = {}, aK = {}, c2 = {};
    if (kv) {
      const lcp kp = (lcp)L + K1_S + w * 8192 + trcb * 4096 + (4 * h + trq) * 32 + trp * 8;
#pragma unroll
      for (int s = 0; s < 8; ++s) {
        const u32x4 wkv = tr_frag(kp, 32, s);
        const u32x4 b = fp ? xf[1][s] : xf[0][s];
        aV = MFMA(b, wkv, aV); aK = MFMA(wkv, b, aK); c2 = MFMA(wkv, ones, c2);
        if (tp == 2 && fp == 0) st16k(rs, wkv, lo, WS_WQKV + (16 * KS_D + 8 * w + s) * 1024);
      }
    }
#pragma unroll
    for (int b = 0; b < 2; ++b)
#pragma unroll
      for (int s = 0; s < 8; ++s)
#pragma unroll
        for (int e = 0; e < 4; ++e) { s1[b] = dot2(xf[b][s][e], 0x3f803f80u, s1[b]); s2[b] = dot2(xf[b][s][e], xf[b][s][e], s2[b]); }
    asm volatile("s_waitcnt vmcnt(0)" ::: "memory");
    STAMP(0, 1);
    {
      const lcp wp = (lcp)L + K1_W + w * 16384 + (4 * h + trq) * 128 + (16 * trcb + 4 * trp) * 2;
#pragma unroll
      for (int s = 0; s < 8; ++s) { wf[0][s] = tr_frag(wp, 128, s); wf[1][s] = tr_frag(wp + 64, 128, s); }
    }
    LAS char* xa = L + K1_X + w * 16384 + lo;
    LAS char* wa = L + K1_W + w * 16384;
#pragma unroll
    for (int f = 0; f < 2; ++f) {
      f32x16 a0 = {}, a1 = {}, c = {};
#pragma unroll
      for (int s = 0; s < 8; ++s) { a0 = MFMA(wf[f][s], xf[0][s], a0); a1 = MFMA(wf[f][s], xf[1][s], a1); c = MFMA(wf[f][s], ones, c); }
      put4(a0, xa + (2 * f) * 4096, 4); put4(a1, xa + (2 * f + 1) * 4096, 4);
      if (f == 0) asm volatile("s_waitcnt lgkmcnt(0)" ::: "memory");
      if (r == 0) {
#pragma unroll
        for (int g = 0; g < 4; ++g) { f32x4 v = {c[4 * g], c[4 * g + 1], c[4 * g + 2], c[4 * g + 3]}; *(LAS f32x4*)(wa + 6144 + f * 128 + g * 32 + h * 16) = v; }
      }
    }
    if (kv) {
      put4(aV, wa + lo, 4); put4(aK, wa + 4096 + lo, 2);
      if (r == 0) {
#pragma unroll
        for (int g = 0; g < 4; ++g) { f32x4 v = {c2[4 * g], c2[4 * g + 1], c2[4 * g + 2], c2[4 * g + 3]}; *(LAS f32x4*)(wa + 6144 + 256 + g * 32 + h * 16) = v; }
      }
    }
#pragma unroll
    for (int b = 0; b < 2; ++b) { const f32x2 sv = {s1[b], s2[b]}; *(LAS f32x2*)(wa + 7168 + b * 512 + lane * 8) = sv; }
    if (tp == 1) {
#pragma unroll
      for (int s = 0; s < 8; ++s) {
        st16k(rs, wf[0][s], lo, WS_WQKV + ((2 * fp) * KS_D + 8 * w + s) * 1024);
        st16k(rs, wf[1][s], lo, WS_WQKV + ((2 * fp + 1) * KS_D + 8 * w + s) * 1024);
      }
    }
  } else {
    unsigned warm = 0;
    {
      unsigned clo, chi;
      if (w == 0) { CODE_ADDR(first_kernel, clo, chi); } else { CODE_ADDR(main_kernel, clo, chi); }
      const char* cp = (const char*)(((unsigned long long)chi << 32) | clo);
      const int off = (w == 0 ? 0 : (w - 1) * 8192) + lane * 128;
      asm volatile("global_load_dword %0, %1, off" : "=v"(warm) : "v"(cp + off) : "memory");
      if ((w == 0 && off + 8192 < FIRST_CODE_BYTES) || (w == 3 && off + 8192 < MAIN_CODE_BYTES))
        asm volatile("global_load_dword %0, %1, off" : "+v"(warm) : "v"(cp + off + 8192) : "memory");
    }
    const unsigned short* src = w1; int ldw = DFF, k0 = 0, c0 = 0, dst = 0, ksn = KS_D;
    if (jw < 56) { src = w1; ldw = DFF; k0 = 256 * (jw & 1) + 64 * w; c0 = 64 * (jw >> 1); dst = WS_W1; ksn = KS_D; }
    else if (jw < 72) { const int b = (jw - 56) * 4 + w; src = wo; ldw = 512; k0 = 64 * (b & 7); c0 = 64 * (b >> 3); dst = WS_WO; ksn = KS_D; }
    else { const int b = (jw - 72) * 4 + w; src = w2; ldw = 512; k0 = 64 * (b >> 3); c0 = 64 * (b & 7); dst = WS_W2; ksn = KS_F; }
    if (jw < 128) {
      const unsigned short* p = src + (size_t)(k0 + (lane >> 3)) * ldw + c0 + (lane & 7) * 8;
#pragma unroll
      for (int i = 0; i < 8; ++i) glds16(p + (size_t)i * 8 * ldw, lds0 + K1_S + w * 8192 + i * 1024);
      asm volatile("s_waitcnt vmcnt(0)" ::: "memory");
      f32x16 c0a = {}, c1a = {};
      const lcp wp = (lcp)L + K1_S + w * 8192 + (4 * h + trq) * 128 + (16 * trcb + 4 * trp) * 2;
      const int d0 = dst + ((c0 >> 5) * ksn + (k0 >> 4)) * 1024;
      u32x4 a[4], b[4];
#pragma unroll
      for (int s = 0; s < 4; ++s) { a[s] = tr_frag(wp, 128, s); b[s] = tr_frag(wp + 64, 128, s); }
#pragma unroll
      for (int s = 0; s < 4; ++s) { st16k(rs, a[s], lo + s * 1024, d0); st16k(rs, b[s], lo + s * 1024, d0 + ksn * 1024); }
      if (jw < 56) {
#pragma unroll
        for (int s = 0; s < 4; ++s) { c0a = MFMA(a[s], ones, c0a); c1a = MFMA(b[s], ones, c1a); }
        if (r == 0) {
          LAS char* cp = L + K1_S + w * 8192 + h * 16;
#pragma unroll
          for (int g = 0; g < 4; ++g) {
            f32x4 u = {c0a[4 * g], c0a[4 * g + 1], c0a[4 * g + 2], c0a[4 * g + 3]}, v = {c1a[4 * g], c1a[4 * g + 1], c1a[4 * g + 2], c1a[4 * g + 3]};
            *(LAS f32x4*)(cp + g * 32) = u; *(LAS f32x4*)(cp + 128 + g * 32) = v;
          }
        }
      }
    }
    asm volatile("s_waitcnt vmcnt(0)" :: "v"(warm) : "memory");
    {
      const u32x4 sv = {SENT, SENT, SENT, SENT};
      const int gw = blockIdx.x * 4 + w;
      st16k(rs, sv, lo, WS_HB + HB_O + gw * 2048); st16k(rs, sv, lo + 1024, WS_HB + HB_O + gw * 2048);
      if (gw < 128) st16k(rs, sv, lo, WS_KV + KV_STR + gw * 1024);
    }
  }
  BAR();
  STAMP(0, 2);
  const int kq = w8 >> 1, ti = w8 & 1;
  float mean, rstd;
  {
    float s1 = 0.f, s2 = 0.f;
#pragma unroll
    for (int sw = 0; sw < 4; ++sw) { const f32x2 v = *(const LAS f32x2*)(L + K1_W + sw * 16384 + 7168 + ti * 512 + lane * 8); s1 += v[0]; s2 += v[1]; }
    s1 = xsum(s1); s2 = xsum(s2);
    mean = s1 * (1.0f / 512.0f);
    float var = __builtin_fmaf(-mean, mean, s2 * (1.0f / 512.0f));
    if (__builtin_expect(__any(!(mean * mean <= 64.0f * var)), 0)) {
      const unsigned short* xp = x + (size_t)(64 * tp + 32 * ti + r) * 512 + 256 * h;
      float m2 = 0.f;
      for (int i = 0; i < 32; ++i) {
        const u32x4 v = *(const u32x4*)(xp + i * 8);
#pragma unroll
        for (int e = 0; e < 4; ++e) { const float a = bflo(v[e]) - mean, b = bfhi(v[e]) - mean; m2 = __builtin_fmaf(a, a, m2); m2 = __builtin_fmaf(b, b, m2); }
      }
      var = xsum(m2) * (1.0f / 512.0f);
    }
    rstd = 1.0f / __builtin_sqrtf(var + 1e-5f);
  }
  if (kv && ti == fp && kq < 3) {
    const int kvo = WS_KV + (2 * tp + fp) * 2048;
    if (kq == 2) {
      float k[8];
#pragma unroll
      for (int g = 0; g < 2; ++g) {
        f32x4 s = {0.f, 0.f, 0.f, 0.f}, c = {0.f, 0.f, 0.f, 0.f};
#pragma unroll
        for (int sw = 0; sw < 4; ++sw) { s += *(const LAS f32x4*)(L + K1_W + sw * 16384 + 4096 + g * 1024 + lo); c += *(const LAS f32x4*)(L + K1_W + sw * 16384 + 6144 + 256 + g * 32 + h * 16); }
#pragma unroll
        for (int e = 0; e < 4; ++e) k[4 * g + e] = __builtin_fmaf(-mean, c[e], s[e]) * rstd;
      }
      const u32x4 ko = {clean(pk2(k[0], k[1])), clean(pk2(k[2], k[3])), clean(pk2(k[4], k[5])), clean(pk2(k[6], k[7]))};
      st16k(rs, ko, lo, kvo);
    } else {
      LAS char* tb = L + K1_S + kq * 512;
      if (h == 0) { const f32x2 m = {mean, rstd}; *(LAS f32x2*)(tb + r * 8) = m; }
      float csv = 0.f;
#pragma unroll
      for (int sw = 0; sw < 4; ++sw) csv += *(const LAS float*)(L + K1_W + sw * 16384 + 6144 + 256 + r * 4);
      if (tp == 2 && fp == 0 && kq == 0 && h == 0) __builtin_amdgcn_raw_buffer_store_b32(__float_as_uint(csv), rs, r * 4, WS_CSQKV + 2048, K1_AUX);
      float v[8];
#pragma unroll
      for (int g2 = 0; g2 < 2; ++g2) {
        f32x4 s = {0.f, 0.f, 0.f, 0.f};
#pragma unroll
        for (int sw = 0; sw < 4; ++sw) s += *(const LAS f32x4*)(L + K1_W + sw * 16384 + (2 * kq + g2) * 1024 + lo);
        const lcp mp = (lcp)tb + (16 * kq + 8 * g2 + 4 * h) * 8;
        const f32x4 m01 = *(const LAS f32x4*)mp, m23 = *(const LAS f32x4*)(mp + 16);
        v[4 * g2] = __builtin_fmaf(-m01[0], csv, s[0]) * m01[1]; v[4 * g2 + 1] = __builtin_fmaf(-m01[2], csv, s[1]) * m01[3];
        v[4 * g2 + 2] = __builtin_fmaf(-m23[0], csv, s[2]) * m23[1]; v[4 * g2 + 3] = __builtin_fmaf(-m23[2], csv, s[3]) * m23[3];
      }
      const u32x4 vo = {clean(pk2(v[0], v[1])), clean(pk2(v[2], v[3])), clean(pk2(v[4], v[5])), clean(pk2(v[6], v[7]))};
      st16k(rs, vo, r >= 16 ? 1024 + kq * 512 + h * 256 + (r - 16) * 16 : 0x30000000, kvo);
    }
  }
  {
    const int fi = kq >> 1, sp = kq & 1;
    u32x4 qf;
    const float sc = rstd * QSCALE;
#pragma unroll
    for (int g = 0; g < 2; ++g) {
      f32x4 s = {0.f, 0.f, 0.f, 0.f}, c = {0.f, 0.f, 0.f, 0.f};
#pragma unroll
      for (int sw = 0; sw < 4; ++sw) {
        s += *(const LAS f32x4*)(L + K1_X + sw * 16384 + (2 * fi + ti) * 4096 + (2 * sp + g) * 1024 + lo);
        c += *(const LAS f32x4*)(L + K1_W + sw * 16384 + 6144 + fi * 128 + (2 * sp + g) * 32 + h * 16);
      }
      qf[2 * g] = pk2(__builtin_fmaf(-mean, c[0], s[0]) * sc, __builtin_fmaf(-mean, c[1], s[1]) * sc);
      qf[2 * g + 1] = pk2(__builtin_fmaf(-mean, c[2], s[2]) * sc, __builtin_fmaf(-mean, c[3], s[3]) * sc);
      if (tp == 1 && ti == 0 && r == 0) st16k(rs, __builtin_bit_cast(u32x4, c), h * 16 + g * 32, WS_CSQKV + 64 * (4 * fp + kq));
    }
    st16k(rs, qf, lo, WS_Q + ((2 * tp + ti) * KS_D + 4 * fp + kq) * 1024);
  }
  if (jw < 56 && w8 == 7) {
    const LAS float* cp = (const LAS float*)(L + K1_S) + lane;
    const float s = (cp[0] + cp[2048]) + (cp[4096] + cp[6144]);
    __builtin_amdgcn_raw_buffer_store_b32(__float_as_uint(s), rs, lane * 4, WS_CS1 + (jw & 1) * 8192 + (jw >> 1) * 256, K1_AUX);
  }
  STAMP(0, 3);
}

DI lcp opaque(lcp p) { unsigned a = (unsigned)(uintptr_t)p; asm volatile("" : "+v"(a)); return (lcp)(uintptr_t)a; }
constexpr int M_A0 = 0, M_B0 = 32768, M_A1 = 65536, M_B1 = 98304;
constexpr int M_MS = 134 * 1024;
constexpr int M_TOP = 136 * 1024;
constexpr int MAIN_LDS = 160 * 1024;
static_assert(M_MS >= KIMG_BYTES + VIMG_BYTES + 2048, "K/V image must fit below the tables");
#define EX(x) __builtin_amdgcn_exp2f(x)

DI void attn_simple(rsrc_t rs, int kvb, const u32x4 qf, f32x16& o, int lane) {
  const int r = lane & 31, h = lane >> 5;
  float tm = -INFINITY;
  for (int t = 0; t < 64; ++t) {
    const u32x4 kx = ld16c(rs, lane * 16, kvb + t * 2048);
    f32x16 z = {};
    f32x16 s = MFMA(kx, qf, z);
#pragma unroll
    for (int i = 0; i < 16; ++i) tm = __builtin_fmaxf(tm, s[i]);
  }
  tm = xmax(tm);
  f32x16 negm;
#pragma unroll
  for (int i = 0; i < 16; ++i) { negm[i] = -tm; o[i] = 0.f; }
  const unsigned cr = r == 16 ? 0x3f803f80u : 0u;
  const u32x4 cv = {cr, cr, cr, cr};
  for (int t = 0; t < 64; ++t) {
    const u32x4 kf = ld16c(rs, lane * 16, kvb + t * 2048);
    u32x4 v0 = ld16c(rs, 1024 + h * 256 + (r & 15) * 16, kvb + t * 2048);
    u32x4 v1 = ld16c(rs, 1536 + h * 256 + (r & 15) * 16, kvb + t * 2048);
    if (r >= 16) { v0 = cv; v1 = cv; }
    f32x16 s = MFMA(kf, qf, negm);
#pragma unroll
    for (int i = 0; i < 16; ++i) s[i] = EX(s[i]);
    u32x4 p0 = {pk2(s[0], s[1]), pk2(s[2], s[3]), pk2(s[4], s[5]), pk2(s[6], s[7])};
    u32x4 p1 = {pk2(s[8], s[9]), pk2(s[10], s[11]), pk2(s[12], s[13]), pk2(s[14], s[15])};
    o = MFMA(v0, p0, o);
    o = MFMA(v1, p1, o);
  }
}

DI void put_tile(const f32x16& a, LAS char* p, int owner_stride) {
#pragma unroll
  for (int g = 0; g < 4; ++g) {
    f32x4 v = {a[4 * g], a[4 * g + 1], a[4 * g + 2], a[4 * g + 3]};
    *(LAS f32x4*)(p + (g >> 1) * owner_stride + (g & 1) * 1024) = v;
  }
}
template <int N> DI void get_half(float (&o)[8], lcp p) {
  f32x4 v[N][2];
#pragma unroll
  for (int s = 0; s < N; ++s) { v[s][0] = *(const LAS f32x4*)(p + s * 2048); v[s][1] = *(const LAS f32x4*)(p + s * 2048 + 1024); }
#pragma unroll
  for (int s = 0; s < N; ++s)
#pragma unroll
    for (int e = 0; e < 4; ++e) { o[e] += v[s][0][e]; o[4 + e] += v[s][1][e]; }
}
DI u32x4 publish_x(rsrc_t rs, const float (&o)[8], int xoff, int stoff, int lane) {
  const int r = lane & 31, h = lane >> 5;
  const u32x4 y = {pk2(o[0], o[1]), pk2(o[2], o[3]), pk2(o[4], o[5]), pk2(o[6], o[7])};
  st16c(rs, y, lane * 16, xoff);
  SBAR();
  float v[8];
#pragma unroll
  for (int i = 0; i < 4; ++i) { v[2 * i] = bflo(y[i]); v[2 * i + 1] = bfhi(y[i]); }
  float s = ((v[0] + v[1]) + (v[2] + v[3])) + ((v[4] + v[5]) + (v[6] + v[7]));
  s = xsum(s);
  const float mu = s * (1.0f / 16.0f);
  float m2 = 0.f;
#pragma unroll
  for (int i = 0; i < 8; ++i) { const float d = v[i] - mu; m2 = __builtin_fmaf(d, d, m2); }
  m2 = xsum(m2);
  const f32x2 st = {s, m2};
  st8c(rs, st, h ? 0x30000000 : r * 16, stoff);
  return y;
}
template <int CTRL> DI float dpp(float v) { return __int_as_float(__builtin_amdgcn_update_dpp(0, __float_as_int(v), CTRL, 0xf, 0xf, true)); }
DI float sum8(float v) { v += dpp<0xB1>(v); v += dpp<0x4E>(v); v += dpp<0x141>(v); return v; }
DI f32x2 combine4(const u32x4 a, const u32x4 b) {
  const float s0 = __uint_as_float(a[0]), s1 = __uint_as_float(a[2]), s2 = __uint_as_float(b[0]), s3 = __uint_as_float(b[2]);
  const float mean = sum8((s0 + s1) + (s2 + s3)) * (1.0f / 512.0f);
  const float d0 = __builtin_fmaf(s0, 1.0f / 16.0f, -mean), d1 = __builtin_fmaf(s1, 1.0f / 16.0f, -mean);
  const float d2 = __builtin_fmaf(s2, 1.0f / 16.0f, -mean), d3 = __builtin_fmaf(s3, 1.0f / 16.0f, -mean);
  const float dd = (d0 * d0 + d1 * d1) + (d2 * d2 + d3 * d3);
  const float m2 = (__uint_as_float(a[1]) + __uint_as_float(a[3])) + (__uint_as_float(b[1]) + __uint_as_float(b[3]));
  const float tot = sum8(__builtin_fmaf(dd, 16.0f, m2));
  f32x2 r = {mean, 1.0f / __builtin_sqrtf(tot * (1.0f / 512.0f) + 1e-5f)};
  return r;
}

constexpr int NW2P = 0;

extern "C" __global__ void __launch_bounds__(512) main_kernel(char* ws, const unsigned short* xin, unsigned short* out) {
  extern __shared__ __attribute__((aligned(16))) char smem[];
  LAS char* L = (LAS char*)smem;
  const rsrc_t rs = ws_rsrc(ws);
  const unsigned lds0 = (unsigned)(uintptr_t)smem;
  const int w = __builtin_amdgcn_readfirstlane(threadIdx.x >> 6);
  const int xcd = blockIdx.x & 7, idx = blockIdx.x >> 3;
  const int tp = 4 * xcd + (idx >> 3), fp = idx & 7;
  const int kq = w >> 1, ti = w & 1;
  const int tt = 2 * tp + ti, og = 4 * fp + kq;
  int lane = threadIdx.x & 63, r = lane & 31, h = lane >> 5, lo = lane * 16;
#define RELANE() do { lane = __builtin_amdgcn_mbcnt_hi(~0u, __builtin_amdgcn_mbcnt_lo(~0u, 0u)); asm volatile("" : "+v"(lane)); lane &= 63; r = lane & 31; h = lane >> 5; lo = lane * 16; } while (0)
  const int own = (tt * KS_D + og) * 1024;
  const int ownst = tt * 8192 + (og >> 1) * 512 + (og & 1) * 8;
  const int ft1 = 7 * fp + (w < 7 ? w : 6);
  const int w1o = w < 7 ? WS_W1 + ft1 * KS_D * 1024 : 0x40000000;
  const int woo = WS_WO + ((2 * fp) * KS_D + 8 * w) * 1024;
  const int w2o = WS_W2 + ((2 * fp) * KS_F + 14 * w) * 1024;
  const int hown = w < 7 ? ((2 * tp) * KS_F + 2 * ft1) * 1024 : 0x20000000;
  const int stw = (2 * tp + (w >> 2)) * 8192 + (8 * (w & 3)) * 16;

  u32x4 qf = ld16(rs, lo, WS_Q + own);
  u32x4 res;
  {
    const unsigned short* xp = xin + (size_t)(32 * tt + r) * D_ + 16 * og + 4 * h;
    const u32x2 a0 = *(const u32x2*)xp, a1 = *(const u32x2*)(xp + 8);
    res = (u32x4){a0[0], a0[1], a1[0], a1[1]};
  }

#pragma unroll 1
  for (int layer = 0; layer < 2; ++layer) {
    const int hb = WS_HB + layer * HB_STR;
    const int KID = 2 + layer;
    STAMP(KID, 0);
    RELANE();
    u32x4 w1f[32], w2f[2][14];
#define W2LOAD(n) w2f[(n) & 1][(n) >> 1] = LDK(sop(w2o + ((n) & 1) * KS_F * 1024), (n) >> 1)
#define DMA(goff, loff) glds16(ws + (size_t)(unsigned)sop(goff) + lo, lds0 + (loff))
    {
      const int kva_ = sop(WS_KV + layer * KV_STR + 4 * w * 1024), kvb_ = sop(WS_KV + layer * KV_STR + (32 + 12 * w) * 1024);
      u32x4 ka[4], kb[12];
#define KVA(i) LDKC(kva_, i)
#define KVB(i) LDKC(kvb_, i)
#pragma unroll
      for (int i = 0; i < 4; ++i) ka[i] = KVA(i);
#pragma unroll
      for (int i = 0; i < 12; ++i) kb[i] = KVB(i);
      SBAR();
      if (h == 0) {
        const unsigned c = (lane & 1) ? 0u : 0x3f803f80u;
        const u32x4 cv = {c, c, c, c};
        *(LAS u32x4*)(L + KIMG_BYTES + (16 * w + (lane >> 1)) * VBLK + 512 + (lane & 1) * 16) = cv;
      }
      LAS char* kdst = L + lo;
      LAS char* vdst = L + KIMG_BYTES + h * VBLK + r * 16;
      {
        VERIFY(4, ka, KVA(i));
#pragma unroll
        for (int i = 0; i < 2; ++i) { *(LAS u32x4*)(kdst + (2 * w + i) * 1024) = ka[2 * i]; *(LAS u32x4*)(vdst + (2 * w + i) * 2 * VBLK) = ka[2 * i + 1]; }
      }
      BAR();
      STAMP(KID, 12);
      const lcp kp0 = (lcp)L + lo;
      const lcp vp0 = (lcp)L + KIMG_BYTES + (r < 16 ? h * 256 + r * 16 : (r > 16) ? 528 : 512);
      lcp kq_ = opaque(kp0), vq = opaque(vp0);
      f32x16 negm, o;
      u32x4 kf = *(lds_u4p)kq_;
      {
        f32x16 z = {};
        f32x16 s = MFMA(kf, qf, z);
        float m = s[0];
#pragma unroll
        for (int i = 1; i < 16; ++i) m = __builtin_fmaxf(m, s[i]);
        m = xmax(m);
#pragma unroll
        for (int i = 0; i < 16; ++i) { negm[i] = -m; o[i] = 0.f; }
        PIN(negm);
      }
      f32x16 sa = MFMA(kf, qf, negm), sb;
      kf = *(lds_u4p)(kq_ + 1024);
      u32x4 v0 = *(lds_u4p)vq;
      u32x4 p1 = {0, 0, 0, 0}, v1p = v0;
      unsigned sv1;
      asm volatile("v_mov_b32 %0, -1" : "=v"(sv1));
      const u32x4 sv4 = {sv1, sv1, sv1, sv1};
      const u32x2 sv2 = {sv4[0], sv4[1]};
      const int fz = layer == 0 ? 0 : 0x40000000;
#define FILL(off, k) do { __builtin_amdgcn_raw_buffer_store_b128(sv4, rs, lo + (k) * 1024, sop(fz + (off)), AUX_SC1); ST_PAD(sv4); } while (0)
#define FILLST(off) __builtin_amdgcn_raw_buffer_store_b64(sv2, rs, h ? 0x30000000 : r * 16, sop(fz + (off) + ownst), AUX_SC1)
      auto side = [&](auto NC) {
        constexpr int n = decltype(NC)::value;
        if constexpr (n == 0) FILL(WS_HB + HB_X1 + own, 0);
        else if constexpr (n < 5) { constexpr int j = n - 1; FILL(WS_HB + HB_H + hown + (j >> 1) * KS_F * 1024, j & 1); }
        else if constexpr (n == 5) FILL(WS_X2 + own, 0);
        else if constexpr (n == 6) FILL(WS_HB + HB_STR + HB_O + own, 0);
        else if constexpr (n == 7) FILL(WS_HB + HB_STR + HB_X1 + own, 0);
        else if constexpr (n < 12) { constexpr int j = n - 8; FILL(WS_HB + HB_STR + HB_H + hown + (j >> 1) * KS_F * 1024, j & 1); }
        else if constexpr (n == 12) FILLST(WS_HB + HB_ST1);
        else if constexpr (n == 13) FILLST(WS_ST2);
        else if constexpr (n == 14) FILLST(WS_HB + HB_STR + HB_ST1);
        else if constexpr (n == 15) { }
        else if constexpr (n < 48) w1f[n - 16] = LDK(sop(w1o), n - 16);
        else if constexpr (n < 56) { constexpr int j = n - 48; DMA(woo + j * 1024, (w >> 2) * M_A1 + ((w & 3) * 8 + j) * 1024); }
        else if constexpr (n < 59) { constexpr int j = 25 + n - 56; DMA(w2o + (j & 1) * KS_F * 1024 + (j >> 1) * 1024, M_TOP + (w * 3 + n - 56) * 1024); }
        else if constexpr (n < 60) { }
        else if constexpr (n < 60 + NW2P) W2LOAD(n - 60);
      };
#define SIDE(n) do { if constexpr ((n) >= 0) side(std::integral_constant<int, (n) < 0 ? 0 : (n)>{}); } while (0)
#define STEP2(sc, sn, koff, voff, N) do { u32x4 p0; \
    sc[0] = EX(sc[0]); sc[1] = EX(sc[1]); sc[2] = EX(sc[2]); sc[3] = EX(sc[3]); sc[4] = EX(sc[4]); SBAR(); \
    sn = MFMA(kf, qf, negm); SBAR(); \
    kf = *(lds_u4p)(kq_ + (koff)); SIDE(N); SBAR(); \
    sc[5] = EX(sc[5]); sc[6] = EX(sc[6]); sc[7] = EX(sc[7]); sc[8] = EX(sc[8]); sc[9] = EX(sc[9]); SBAR(); \
    o = MFMA(v1p, p1, o); SBAR(); \
    v1p = *(lds_u4p)(vq + (voff) - VBLK); SBAR(); \
    p0[0] = pk2(sc[0], sc[1]); p0[1] = pk2(sc[2], sc[3]); p0[2] = pk2(sc[4], sc[5]); p0[3] = pk2(sc[6], sc[7]); SBAR(); \
    sc[10] = EX(sc[10]); sc[11] = EX(sc[11]); sc[12] = EX(sc[12]); sc[13] = EX(sc[13]); sc[14] = EX(sc[14]); sc[15] = EX(sc[15]); SBAR(); \
    o = MFMA(v0, p0, o); SBAR(); \
    v0 = *(lds_u4p)(vq + (voff)); SIDE((N) < 0 ? -1 : (N) + 1); SBAR(); \
    p1[0] = pk2(sc[8], sc[9]); p1[1] = pk2(sc[10], sc[11]); p1[2] = pk2(sc[12], sc[13]); p1[3] = pk2(sc[14], sc[15]); SBAR(); } while (0)
#define STEP4(N) do { \
      STEP2(sa, sb, 2 * 1024, 2 * VBLK, N); \
      STEP2(sb, sa, 3 * 1024, 4 * VBLK, (N) < 0 ? -1 : (N) + 2); \
      STEP2(sa, sb, 4 * 1024, 6 * VBLK, (N) < 0 ? -1 : (N) + 4); \
      STEP2(sb, sa, 5 * 1024, 8 * VBLK, (N) < 0 ? -1 : (N) + 6); \
      kq_ += 4096; vq += 8 * VBLK; } while (0)
#pragma unroll 1
      for (int t = 0; t < 8; t += 4) STEP4(-1);
      {
        VERIFY(12, kb, KVB(i));
#pragma unroll
        for (int i = 0; i < 6; ++i) { *(LAS u32x4*)(kdst + (16 + 6 * w + i) * 1024) = kb[2 * i]; *(LAS u32x4*)(vdst + (16 + 6 * w + i) * 2 * VBLK) = kb[2 * i + 1]; }
      }
      BAR();
      STAMP(KID, 13);
      STEP4(0); STEP4(8); STEP4(16); STEP4(24); STEP4(32); STEP4(40);
      STEP4(-1);
      BAR();
      STEP4(48); STEP4(56);
#pragma unroll 1
      for (int t = 44; t < 64; t += 4) STEP4(-1);
      o = MFMA(v1p, p1, o);
      STAMP(KID, 14);
      float l = lowhalf(o[8]);
      bool bad = !(l < 1.0e18f);
#pragma unroll
      for (int i = 0; i < 8; ++i) bad |= !(__builtin_fabsf(o[i]) < 3.0e38f);
      if (__builtin_expect(__any(bad), 0)) {
        attn_simple(rs, WS_KV + layer * KV_STR, qf, o, lane);
        l = lowhalf(o[8]);
      }
      const float rl = 1.0f / l;
      const u32x4 oa = {pk2(o[0] * rl, o[1] * rl), pk2(o[2] * rl, o[3] * rl), pk2(o[4] * rl, o[5] * rl), pk2(o[6] * rl, o[7] * rl)};
      asm volatile("s_waitcnt vmcnt(0)" ::: "memory");
      st16c(rs, oa, lo, sop(hb + HB_O + own));
      BAR();
    }
    STAMP(KID, 15);
    RELANE();
    {
      u32x4 af[8], wo0[8], wo1[8];
      const lcp wp = (lcp)L + 8 * kq * 1024 + lo;
#pragma unroll
      for (int i = 0; i < 8; ++i) { wo0[i] = *(lds_u4p)(wp + M_A0 + i * 1024); PIN(wo0[i]); }
#pragma unroll
      for (int i = 0; i < 2; ++i) { wo1[i] = *(lds_u4p)(opaque(wp + M_A1) + i * 1024); PIN(wo1[i]); }
      const int aa_ = sop(hb + HB_O + (tt * KS_D + 8 * kq) * 1024);
      HANDOFF_LOAD(8, af, LDKC(aa_, i), fold4(m_, af[i]));
      STAMP(KID, 1);
      f32x16 a0 = {}, a1 = {};
      {
        const lcp wp1 = opaque(wp + M_A1);
#pragma unroll
        for (int i = 0; i < 8; ++i) {
          a0 = MFMA(wo0[i], af[i], a0);
          if (i < 6) wo1[2 + i] = *(lds_u4p)(wp1 + (2 + i) * 1024);
          SBAR();
        }
#pragma unroll
        for (int i = 0; i < 8; ++i) {
          a1 = MFMA(wo1[i], af[i], a1);
          if (i == 1) put_tile(a0, L + M_B0 + (ti * 4 + kq) * 2048 + lo, 2 * 8192);
          SBAR();
        }
        put_tile(a1, L + M_B1 + (ti * 4 + kq) * 2048 + lo, 2 * 8192);
      }
      BAR();
      float o[8];
#pragma unroll
      for (int i = 0; i < 4; ++i) { o[2 * i] = bflo(res[i]); o[2 * i + 1] = bfhi(res[i]); }
      get_half<4>(o, (lcp)L + M_B0 + (w >> 2) * 65536 + (w & 3) * 8192 + lo);
      (void)publish_x(rs, o, sop(hb + HB_X1 + own), sop(hb + HB_ST1 + ownst), lane);
      SBAR();
    }
    STAMP(KID, 2);
    RELANE();
    f32x4 cs[4], c2[4];
#pragma unroll
    for (int g = 0; g < 4; ++g) { cs[g] = __builtin_bit_cast(f32x4, ld16(rs, h * 16 + g * 32, sop(WS_CS1 + 128 * ft1))); c2[g] = __builtin_bit_cast(f32x4, ld16(rs, h * 16 + g * 32, sop(WS_CS1 + 8192 + 128 * ft1))); }
#pragma unroll
    for (int n = 0; n < 4; ++n) W2LOAD(n);
    SBAR();
    STAMP(KID, 3);
    {
      u32x4 xf[8];
      const int xo_ = sop(hb + HB_X1 + ((2 * tp) * KS_D + 8 * w) * 1024);
      HANDOFF_LOAD(8, xf, LDKC(xo_, i), fold4(m_, xf[i]));
#pragma unroll
      for (int i = 0; i < 8; ++i) *(LAS u32x4*)(L + (w >> 2) * M_A1 + (8 * (w & 3) + i) * 1024 + lo) = xf[i];
#pragma unroll
      for (int g = 0; g < 4; ++g) { cs[g] += c2[g]; PIN(cs[g]); }
      BAR();
    }
    STAMP(KID, 4);
    u32x4 h0[14], h1[14];
    const int h0_ = hb + HB_H + ((2 * tp) * KS_F + 14 * w) * 1024;
#define H0LOAD(i) LDKC(sop(h0_), i)
#define H1LOAD(i) LDKC(sop(h0_ + KS_F * 1024), i)
    {
      const lcp xp = (lcp)L + lo;
#define XF(i) (*(lds_u4p)(xp + ((i) >> 5) * M_A1 + ((i) & 31) * 1024))
      auto epi = [&](const f32x16& o, f32x2 ms, int hoff) {
        const float nm = -ms[0], sc = ms[1];
        u32x4 e0, e1;
#pragma unroll
        for (int g = 0; g < 4; ++g) {
          const float a = __builtin_fmaxf(__builtin_fmaf(nm, cs[g][0], o[4 * g]) * sc, 0.f), b = __builtin_fmaxf(__builtin_fmaf(nm, cs[g][1], o[4 * g + 1]) * sc, 0.f);
          const float c = __builtin_fmaxf(__builtin_fmaf(nm, cs[g][2], o[4 * g + 2]) * sc, 0.f), d = __builtin_fmaxf(__builtin_fmaf(nm, cs[g][3], o[4 * g + 3]) * sc, 0.f);
          if (g < 2) { e0[2 * g] = pk2(a, b); e0[2 * g + 1] = pk2(c, d); }
          else { e1[2 * (g - 2)] = pk2(a, b); e1[2 * (g - 2) + 1] = pk2(c, d); }
        }
        const int eo_ = sop(hb + HB_H + hoff);
        st16c(rs, e0, lo, eo_); st16c(rs, e1, lo + 1024, eo_);
      };
      f32x16 a0 = {};
      u32x4 st[2];
      const int so_ = sop(hb + HB_ST1 + stw);
#define STLOAD(i) ld16c(rs, (lane & 7) * 1024 + (lane >> 3) * 16 + (i) * 512, so_)
      st[0] = STLOAD(0); st[1] = STLOAD(1);
      constexpr int PF = 4;
      u32x4 xa[PF];
#pragma unroll
      for (int i = 0; i < PF; ++i) xa[i] = XF(i);
#pragma unroll
      for (int i = 0; i < 32; ++i) {
        a0 = MFMA(w1f[i], xa[i % PF], a0);
        xa[i % PF] = XF(i + PF);
        if ((i & 1) == 0 && i < 16) DMA(w2o + ((i >> 1) & 1) * KS_F * 1024 + (4 + (i >> 2)) * 1024, M_B0 + (w >> 2) * 65536 + ((w & 3) * 8 + (i >> 1)) * 1024);
        SBAR();
      }
      PIN(a0);
      VERIFY(2, st, STLOAD(i));
      {
        const f32x2 ms = combine4(st[0], st[1]);
        if ((lane & 7) == 0) *(LAS f32x2*)(L + M_MS + (8 * w + (lane >> 3)) * 8) = ms;
      }
      asm volatile("s_waitcnt vmcnt(0)" ::: "memory");
      BAR();
      STAMP(KID, 11);
      const f32x2 ms0 = *(const LAS f32x2*)(L + M_MS + r * 8), ms1 = *(const LAS f32x2*)(L + M_MS + (32 + r) * 8);
      epi(a0, ms0, hown);
      SBAR();
      f32x16 a1 = {};
#pragma unroll
      for (int i = 0; i < 32; ++i) {
        a1 = MFMA(w1f[i], xa[i % PF], a1);
        if (i + PF < 32) xa[i % PF] = XF(32 + i + PF);
        if (i >= 1 && i < 5) W2LOAD(3 + i);
        if (i >= 5 && i < 14) W2LOAD(11 + i);
        if (i >= 18) h0[i - 18] = H0LOAD(i - 18);
        SBAR();
      }
      PIN(a1);
      epi(a1, ms1, hown + KS_F * 1024);
      SBAR();
    }
    STAMP(KID, 5);
    RELANE();
    u32x4 wq[2][8], wkv[8];
    float o[8];
    {
      const int wqo = WS_WQKV + ((2 * fp) * KS_D + 8 * kq) * 1024;
      LAS char* rp = L + w * 2048 + lo;
#pragma unroll
      for (int n = 8; n < 16; ++n) w2f[n & 1][n >> 1] = *(lds_u4p)((lcp)L + M_B0 + (w >> 2) * 65536 + ((w & 3) * 8 + n - 8) * 1024 + lo);
      w2f[1][12] = *(lds_u4p)((lcp)L + M_TOP + (w * 3) * 1024 + lo);
      w2f[0][13] = *(lds_u4p)((lcp)L + M_TOP + (w * 3 + 1) * 1024 + lo);
      w2f[1][13] = *(lds_u4p)((lcp)L + M_TOP + (w * 3 + 2) * 1024 + lo);
      const u32x4 y1 = *(lds_u4p)((lcp)L + ti * M_A1 + og * 1024 + lo);
      BAR();
      {
        VERIFY(14, h0, H0LOAD(i));
      }
      f32x16 a0 = {}, a1 = {};
#pragma unroll
      for (int i = 0; i < 14; ++i) {
        a0 = MFMA(w2f[0][i], h0[i], a0);
        a1 = MFMA(w2f[1][i], h0[i], a1);
        if (i >= 4) { h1[i - 4] = H1LOAD(i - 4); if (i >= 10) h1[i] = H1LOAD(i); }
        SBAR();
      }
      put_tile(a0, rp + M_B0, 16384);
      put_tile(a1, rp + M_B1, 16384);
      STAMP(KID, 6);
      {
        VERIFY(14, h1, H1LOAD(i));
      }
      f32x16 b0 = {}, b1 = {};
#pragma unroll
      for (int i = 0; i < 14; ++i) {
        b0 = MFMA(w2f[0][i], h1[i], b0);
        if (i < 8) wq[0][i] = LDK(sop(wqo), i);
        SBAR();
      }
      put_tile(b0, rp + M_A0, 16384);
#pragma unroll
      for (int i = 0; i < 14; ++i) {
        b1 = MFMA(w2f[1][i], h1[i], b1);
        if (i < 8) wq[1][i] = LDK(sop(wqo + KS_D * 1024), i);
        SBAR();
      }
      put_tile(b1, rp + M_A1, 16384);
      STAMP(KID, 7);
      BAR();
      RELANE();
#pragma unroll
      for (int i = 0; i < 4; ++i) { o[2 * i] = bflo(y1[i]); o[2 * i + 1] = bfhi(y1[i]); }
      get_half<8>(o, (lcp)L + (ti ? M_A0 : M_B0) + (kq >> 1) * 65536 + (kq & 1) * 16384 + lo);
    }
    if (layer == 1) {
      const u32x2 a0 = {pk2(o[0], o[1]), pk2(o[2], o[3])}, a1 = {pk2(o[4], o[5]), pk2(o[6], o[7])};
      unsigned short* yp = out + (size_t)(32 * tt + r) * D_ + 16 * og + 4 * h;
      *(u32x2*)(yp) = a0; *(u32x2*)(yp + 8) = a1;
      STAMP(KID, 8);
      return;
    }
    res = publish_x(rs, o, sop(WS_X2 + own), sop(WS_ST2 + ownst), lane);
    SBAR();
    const bool kvw = fp < 2 && ti == fp;
    f32x4 cq[2], csk[2];
#pragma unroll
    for (int i = 0; i < 8; ++i) wkv[i] = LDK(sop(kvw ? WS_WQKV + (16 * KS_D + 8 * kq) * 1024 : 0x40000000), i);
#pragma unroll
    for (int g = 0; g < 2; ++g) cq[g] = __builtin_bit_cast(f32x4, ld16(rs, h * 16 + g * 32, sop(WS_CSQKV + 64 * og)));
#pragma unroll
    for (int g = 0; g < 2; ++g) csk[g] = __builtin_bit_cast(f32x4, ld16(rs, h * 16 + g * 32, WS_CSQKV + 2048));
    const float csv = __builtin_bit_cast(float, __builtin_amdgcn_raw_buffer_load_b32(rs, r * 4, WS_CSQKV + 2048, 0));
    SBAR();
    STAMP(KID, 8);
    {
      u32x4 xf[8], st[2];
      const int xa_ = sop(WS_X2 + (tt * KS_D + 8 * kq) * 1024), so_ = sop(WS_ST2 + stw);
      int spins = 0;
      for (;;) {
        asm volatile("" ::: "memory");
#pragma unroll
        for (int i = 0; i < 8; ++i) xf[i] = LDKC(xa_, i);
        st[0] = STLOAD(0); st[1] = STLOAD(1);
        unsigned m = fold4(fold4(0u, st[0]), st[1]);
#pragma unroll
        for (int i = 0; i < 8; ++i) m = fold4(m, xf[i]);
        if (!__any(m == SENT) || ++spins > SPIN_CAP) break;
      }
      STAMP(KID, 9);
      f32x16 aK = {}, aV = {};
      if (kvw) {
#pragma unroll
        for (int i = 0; i < 8; ++i) { aV = MFMA(xf[i], wkv[i], aV); aK = MFMA(wkv[i], xf[i], aK); }
      }
      f32x16 a0 = {}, a1 = {};
#pragma unroll
      for (int i = 0; i < 8; ++i) { a0 = MFMA(wq[0][i], xf[i], a0); a1 = MFMA(wq[1][i], xf[i], a1); }
      {
        const f32x2 msw = combine4(st[0], st[1]);
        if ((lane & 7) == 0) *(LAS f32x2*)(L + M_MS + (8 * w + (lane >> 3)) * 8) = msw;
      }
      BAR();
      if (kvw) {
        LAS char* kp = L + M_TOP + kq * 6144 + lo;
#pragma unroll
        for (int g = 0; g < 4; ++g) { f32x4 u = {aV[4 * g], aV[4 * g + 1], aV[4 * g + 2], aV[4 * g + 3]}; *(LAS f32x4*)(kp + g * 1024) = u; }
#pragma unroll
        for (int g = 0; g < 2; ++g) { f32x4 u = {aK[4 * g], aK[4 * g + 1], aK[4 * g + 2], aK[4 * g + 3]}; *(LAS f32x4*)(kp + (4 + g) * 1024) = u; }
      }
      put_tile(a0, L + M_B0 + (ti * 4 + kq) * 2048 + lo, 2 * 8192);
      put_tile(a1, L + M_B1 + (ti * 4 + kq) * 2048 + lo, 2 * 8192);
      BAR();
      if (fp < 2 && ti != fp && kq < 3) {
        const int kvo = sop(WS_KV + KV_STR + (2 * tp + fp) * 2048);
        const lcp kp = (lcp)L + M_TOP + lo;
        if (kq == 2) {
          const f32x2 mk = *(const LAS f32x2*)(L + M_MS + (32 * fp + r) * 8);
          const float mu = mk[0], rsd = mk[1];
          float k[8];
#pragma unroll
          for (int g = 0; g < 2; ++g) {
            f32x4 s = {0.f, 0.f, 0.f, 0.f};
#pragma unroll
            for (int sw = 0; sw < 4; ++sw) s += *(const LAS f32x4*)(kp + sw * 6144 + (4 + g) * 1024);
#pragma unroll
            for (int e = 0; e < 4; ++e) k[4 * g + e] = __builtin_fmaf(-mu, csk[g][e], s[e]) * rsd;
          }
          const u32x4 ko = {pk2(k[0], k[1]), pk2(k[2], k[3]), pk2(k[4], k[5]), pk2(k[6], k[7])};
          st16c(rs, ko, lo, kvo);
        } else {
          float v[8];
#pragma unroll
          for (int g2 = 0; g2 < 2; ++g2) {
            f32x4 s = {0.f, 0.f, 0.f, 0.f};
#pragma unroll
            for (int sw = 0; sw < 4; ++sw) s += *(const LAS f32x4*)(kp + sw * 6144 + (2 * kq + g2) * 1024);
            const lcp mp = (lcp)L + M_MS + (32 * fp + 16 * kq + 8 * g2 + 4 * h) * 8;
            const f32x4 m01 = *(const LAS f32x4*)mp, m23 = *(const LAS f32x4*)(mp + 16);
            v[4 * g2] = __builtin_fmaf(-m01[0], csv, s[0]) * m01[1]; v[4 * g2 + 1] = __builtin_fmaf(-m01[2], csv, s[1]) * m01[3];
            v[4 * g2 + 2] = __builtin_fmaf(-m23[0], csv, s[2]) * m23[1]; v[4 * g2 + 3] = __builtin_fmaf(-m23[2], csv, s[3]) * m23[3];
          }
          const u32x4 vo = {pk2(v[0], v[1]), pk2(v[2], v[3]), pk2(v[4], v[5]), pk2(v[6], v[7])};
          st16c(rs, vo, r >= 16 ? 1024 + kq * 512 + h * 256 + (r - 16) * 16 : 0x30000000, kvo);
        }
      }
      float q[8] = {0.f, 0.f, 0.f, 0.f, 0.f, 0.f, 0.f, 0.f};
      get_half<4>(q, (lcp)L + M_B0 + (w >> 2) * 65536 + (w & 3) * 8192 + lo);
      const f32x2 ms = *(const LAS f32x2*)(L + M_MS + (32 * ti + r) * 8);
      const float nm = -ms[0], sc = ms[1] * QSCALE;
#pragma unroll
      for (int g = 0; g < 2; ++g) {
        qf[2 * g] = pk2(__builtin_fmaf(nm, cq[g][0], q[4 * g]) * sc, __builtin_fmaf(nm, cq[g][1], q[4 * g + 1]) * sc);
        qf[2 * g + 1] = pk2(__builtin_fmaf(nm, cq[g][2], q[4 * g + 2]) * sc, __builtin_fmaf(nm, cq[g][3], q[4 * g + 3]) * sc);
      }
      STAMP(KID, 10);
      BAR();
    }
  }
}

extern "C" void kernel_launch(void* const* d_in, const int* in_sizes, int n_in, void* d_out, int out_size, void* d_ws, size_t ws_size,
                              hipStream_t stream) {
  const unsigned short* x = (const unsigned short*)d_in[0];
  const unsigned short* wq = (const unsigned short*)d_in[1];
  const unsigned short* wk = (const unsigned short*)d_in[2];
  const unsigned short* wv = (const unsigned short*)d_in[3];
  const unsigned short* wo = (const unsigned short*)d_in[4];
  const unsigned short* w1 = (const unsigned short*)d_in[6];
  const unsigned short* w2 = (const unsigned short*)d_in[7];
  char* ws = (char*)d_ws;
  (void)hipFuncSetAttribute((const void*)first_kernel, hipFuncAttributeMaxDynamicSharedMemorySize, K1_LDS);
  (void)hipFuncSetAttribute((const void*)main_kernel, hipFuncAttributeMaxDynamicSharedMemorySize, MAIN_LDS);
  first_kernel<<<256, 512, K1_LDS, stream>>>(x, wq, wk, wv, wo, w1, w2, ws);
  main_kernel<<<256, 512, MAIN_LDS, stream>>>(ws, x, (unsigned short*)d_out);
}
```

```cpp
#include <hip/hip_runtime.h>
#include <hip/hip_bf16.h>
#include <stdint.h>
#include <type_traits>

typedef __attribute__((ext_vector_type(8))) __bf16 bf16x8;
typedef __attribute__((ext_vector_type(2))) __bf16 bf16x2;
typedef __attribute__((ext_vector_type(16))) float f32x16;
typedef __attribute__((ext_vector_type(4))) float f32x4;
typedef __attribute__((ext_vector_type(2))) float f32x2;
typedef __attribute__((ext_vector_type(4))) unsigned u32x4;
typedef __attribute__((ext_vector_type(2))) unsigned u32x2;
#define DI __device__ __forceinline__
#define MFMA(a, b, c) __builtin_amdgcn_mfma_f32_32x32x16_bf16(__builtin_bit_cast(bf16x8, (a)), __builtin_bit_cast(bf16x8, (b)), (c), 0, 0, 0)
#define SBAR() __builtin_amdgcn_sched_barrier(0)
#define PIN(x) asm volatile("" : "+v"(x))
#if defined(__HIP_DEVICE_COMPILE__)
#define NOPK __attribute__((target("no-packed-fp32-ops")))
#else
#define NOPK
#endif
#define AI __attribute__((always_inline))
#define ANY(x) (__builtin_amdgcn_ballot_w64(x) != 0ull)
#define F2U(x) __builtin_bit_cast(unsigned, (float)(x))
#define U2F(x) __builtin_bit_cast(float, (unsigned)(x))
#define LAS __attribute__((address_space(3)))

constexpr int S_ = 2048, D_ = 512, DFF = 1792;
constexpr int NTT = S_ / 32;
constexpr int KS_D = D_ / 16;
constexpr int KS_F = DFF / 16;
constexpr int VSTR = 1056;
constexpr int KIMG_BYTES = NTT * 1024;
constexpr float QSCALE = 0.25f * 1.4426950408889634f;

constexpr int MB2 = 2 * 1024 * 1024;
constexpr int WS_WQKV = 0;
constexpr int WS_WO   = WS_WQKV + 17 * 32 * 1024;
constexpr int WS_W1   = WS_WO + 16 * 32 * 1024;
constexpr int WS_W2   = WS_W1 + 56 * 32 * 1024;
constexpr int WS_CSQKV = WS_W2 + 16 * 112 * 1024;
constexpr int WS_CS1  = WS_CSQKV + 4096;
constexpr int WS_Q    = WS_CS1 + 16384;
constexpr int WS_KV   = WS_Q + MB2;
constexpr int KV_STR  = 128 * 1024;
constexpr int WS_O    = WS_KV + 2 * KV_STR;
constexpr int WS_X1   = WS_O + 2 * MB2;
constexpr int WS_ST1  = WS_X1 + MB2;
constexpr int WS_H    = WS_ST1 + 512 * 1024;
constexpr int WS_X2   = WS_H + S_ * DFF * 2;
constexpr int WS_ST2  = WS_X2 + MB2;
constexpr int WS_END  = WS_ST2 + 512 * 1024;

DI unsigned pk2(float a, float b) { f32x2 v = {a, b}; bf16x2 r = __builtin_convertvector(v, bf16x2); return __builtin_bit_cast(unsigned, r); }
DI float bflo(unsigned u) { return U2F(u << 16); }
DI float bfhi(unsigned u) { return U2F(u & 0xffff0000u); }
DI float xsum(float v) {
  auto rr = __builtin_amdgcn_permlane32_swap(F2U(v), F2U(v), false, false);
  return U2F(rr[0]) + U2F(rr[1]);
}
DI float xmax(float v) {
  auto rr = __builtin_amdgcn_permlane32_swap(F2U(v), F2U(v), false, false);
  return __builtin_fmaxf(U2F(rr[0]), U2F(rr[1]));
}
DI float lowhalf(float v) {
  auto rr = __builtin_amdgcn_permlane32_swap(F2U(v), F2U(v), false, false);
  return U2F(rr[0]);
}

typedef __amdgpu_buffer_rsrc_t rsrc_t;
constexpr int AUX_SC1 = 16;
DI rsrc_t ws_rsrc(char* ws) { return __builtin_amdgcn_make_buffer_rsrc((void*)ws, 0, WS_END, 0x00020000); }
DI u32x4 ld16(rsrc_t rs, int voff, int soff) { return __builtin_amdgcn_raw_buffer_load_b128(rs, voff, soff, 0); }
DI u32x4 ld16c(rsrc_t rs, int voff, int soff) { return __builtin_amdgcn_raw_buffer_load_b128(rs, voff, soff, AUX_SC1); }
#define ST_PAD(v) asm volatile("s_nop 1" :: "v"(v) : "memory")
DI int sop(int x) { asm volatile("" : "+s"(x)); return x; }
#define LDK(base, k) ld16(rs, lo + ((k) & 3) * 1024, (base) + ((k) >> 2) * 4096)
#define LDKC(base, k) ld16c(rs, lo + ((k) & 3) * 1024, (base) + ((k) >> 2) * 4096)
constexpr unsigned SENT = 0xffffffffu;
DI unsigned clean(unsigned u) { return u < SENT ? u : SENT - 1u; }
DI void st16c(rsrc_t rs, u32x4 v, int voff, int soff) {
  u32x4 c = {clean(v[0]), clean(v[1]), clean(v[2]), clean(v[3])};
  __builtin_amdgcn_raw_buffer_store_b128(c, rs, voff, soff, AUX_SC1);
  ST_PAD(c);
}
DI void st8c(rsrc_t rs, f32x2 v, int voff, int soff) {
  u32x2 c = {clean(F2U(v[0])), clean(F2U(v[1]))};
  __builtin_amdgcn_raw_buffer_store_b64(c, rs, voff, soff, AUX_SC1);
}
DI unsigned umax3(unsigned a, unsigned b, unsigned c) { unsigned m = a > b ? a : b; return m > c ? m : c; }
DI unsigned fold4(unsigned m, u32x4 v) { return umax3(umax3(m, v[0], v[1]), v[2], v[3]); }
constexpr int SPIN_CAP = 1 << 15;

#define HANDOFF_LOAD(ID, N, DST, LOADEXPR, FOLD) do { \
    int spins_ = 0; \
    for (;;) { \
      asm volatile("" ::: "memory"); \
      _Pragma("unroll") for (int i = 0; i < (N); ++i) DST[i] = LOADEXPR; \
      unsigned m_ = 0; \
      _Pragma("unroll") for (int i = 0; i < (N); ++i) m_ = FOLD; \
      if (!ANY(m_ == SENT) || ++spins_ > SPIN_CAP) break; \
      __builtin_amdgcn_s_sleep(2); \
    } } while (0)

#define VERIFY(ID, N, DST, LOADEXPR) do { \
    int spins_ = 0; \
    _Pragma("unroll") for (int i = 0; i < (N); ++i) asm volatile("" : "+v"(DST[i]));        \
    _Pragma("nounroll") for (;;) { \
      unsigned m_ = 0; \
      _Pragma("unroll") for (int i = 0; i < (N); ++i) m_ = fold4(m_, DST[i]); \
      if (!ANY(m_ == SENT) || ++spins_ > SPIN_CAP) break; \
      __builtin_amdgcn_s_sleep(1); \
      asm volatile("" ::: "memory"); \
      _Pragma("unroll") for (int i = 0; i < (N); ++i) DST[i] = LOADEXPR; \
      _Pragma("unroll") for (int i = 0; i < (N); ++i) asm volatile("" : "+v"(DST[i])); \
    } } while (0)

#define BAR() do { asm volatile("s_waitcnt lgkmcnt(0)" ::: "memory"); __builtin_amdgcn_s_barrier(); asm volatile("" ::: "memory"); } while (0)

typedef LAS const u32x4* lds_u4p;
typedef const LAS char* lcp;
typedef short s16x4 __attribute__((ext_vector_type(4)));
DI void glds16(const void* g, unsigned lds_base) {
  unsigned sv;
  asm volatile("s_mov_b32 %0, m0\n\ts_mov_b32 m0, %2\n\ts_nop 0\n\tglobal_load_lds_dwordx4 %1, off\n\ts_mov_b32 m0, %0"
               : "=&s"(sv) : "v"(g), "s"(lds_base) : "memory");
}
DI u32x2 ldtr(lcp p) { return __builtin_bit_cast(u32x2, __builtin_amdgcn_ds_read_tr16_b64_v4i16((LAS s16x4*)p)); }
DI u32x4 tr_frag(lcp p, int rowbytes, int s) {
  const u32x2 a = ldtr(p + 16 * s * rowbytes), b = ldtr(p + (16 * s + 8) * rowbytes);
  u32x4 o = {a[0], a[1], b[0], b[1]};
  return o;
}
constexpr int FIRST_CODE_BYTES = 11264, MAIN_CODE_BYTES = 32768;
constexpr int K1_X = 0, K1_W = 65536, K1_S = 131072, K1_LDS = 163840;
#define K1_AUX 16
DI void st16k(rsrc_t rs, u32x4 v, int voff, int soff) { __builtin_amdgcn_raw_buffer_store_b128(v, rs, voff, soff, K1_AUX); ST_PAD(v); }
DI float dot2(unsigned a, unsigned b, float c) { return __builtin_amdgcn_fdot2_f32_bf16(__builtin_bit_cast(bf16x2, a), __builtin_bit_cast(bf16x2, b), c, false); }
DI void put4(const f32x16& a, LAS char* p, int n) {
#pragma unroll
  for (int g = 0; g < 4; ++g) if (g < n) { f32x4 v = {a[4 * g], a[4 * g + 1], a[4 * g + 2], a[4 * g + 3]}; *(LAS f32x4*)(p + g * 1024) = v; }
}

#define CODE_ADDR(sym, lo32, hi32) asm volatile("s_getpc_b64 s[40:41]\n\ts_add_u32 s40, s40, " #sym "@rel32@lo+4\n\ts_addc_u32 s41, s41, " #sym "@rel32@hi+12\n\ts_mov_b32 %0, s40\n\ts_mov_b32 %1, s41" \
    : "=s"(lo32), "=s"(hi32) :: "s40", "s41", "scc")
#define CODE_WARM 1

extern "C" __global__ void NOPK __launch_bounds__(512, 2) first_kernel(const unsigned short* x, const unsigned short* wq, const unsigned short* wk,
                                                    const unsigned short* wv, const unsigned short* wo, const unsigned short* w1,
                                                    const unsigned short* w2, char* ws) {
  extern __shared__ __attribute__((aligned(16))) char smem[];
  LAS char* L = (LAS char*)smem;
  const unsigned lds0 = (unsigned)(uintptr_t)smem;
  const rsrc_t rs = ws_rsrc(ws);
  const int lane = threadIdx.x & 63, r = lane & 31, h = lane >> 5, lo = lane * 16;
  const int w8 = __builtin_amdgcn_readfirstlane(threadIdx.x >> 6), w = w8 & 3;
  const int xcd = blockIdx.x & 7, idx = blockIdx.x >> 3;
  const int tp = 4 * xcd + (idx >> 3), fp = idx & 7;
  const bool kv = fp < 2;
  const int trq = (lane & 15) >> 2, trp = lane & 3, trcb = (lane >> 4) & 1;
  const u32x4 ones = {0x3f803f80u, 0x3f803f80u, 0x3f803f80u, 0x3f803f80u};
  const int jw = kv ? 192 : (fp - 2) * 32 + tp;
  if (w8 < 4) {
    {
      const unsigned short* wsrc = wq + (size_t)(128 * w + (lane >> 3)) * 512 + 64 * fp + (lane & 7) * 8;
      const int xr = lane >> 4;
      if (kv) {
#pragma unroll
        for (int i = 0; i < 4; ++i) {
          glds16(wk + (size_t)128 * w * 16 + i * 512 + lane * 8, lds0 + K1_S + w * 8192 + i * 1024);
          glds16(wv + (size_t)128 * w * 16 + i * 512 + lane * 8, lds0 + K1_S + w * 8192 + 4096 + i * 1024);
        }
      }
#pragma unroll
      for (int i = 0; i < 16; ++i) {
        const int row = 4 * i + xr;
        glds16(x + (size_t)(64 * tp + row) * 512 + 128 * w + (((lane & 15) ^ (row & 15)) * 8), lds0 + K1_X + w * 16384 + i * 1024);
      }
#pragma unroll
      for (int i = 0; i < 16; ++i) glds16(wsrc + (size_t)i * 8 * 512, lds0 + K1_W + w * 16384 + i * 1024);
    }
    asm volatile("s_waitcnt vmcnt(16)" ::: "memory");
    u32x4 wf[2][8], xf[2][8];
    float s1[2] = {0.f, 0.f}, s2[2] = {0.f, 0.f};
#pragma unroll
    for (int b = 0; b < 2; ++b) {
      const lcp xp = (lcp)L + K1_X + w * 16384 + (32 * b + r) * 256 + h * 8;
#pragma unroll
      for (int s = 0; s < 8; ++s) {
        const u32x2 a = *(const LAS u32x2*)(xp + (((2 * s) ^ (r & 15)) * 16)), c = *(const LAS u32x2*)(xp + (((2 * s + 1) ^ (r & 15)) * 16));
        xf[b][s] = (u32x4){a[0], a[1], c[0], c[1]};
      }
    }
    f32x16 aV = {}, aK = {};
    float c2 = 0.f;
    if (kv) {
      const lcp kp = (lcp)L + K1_S + w * 8192 + trcb * 4096 + (4 * h + trq) * 32 + trp * 8;
#pragma unroll
      for (int s = 0; s < 8; ++s) {
        const u32x4 wkv = tr_frag(kp, 32, s);
        const u32x4 b = fp ? xf[1][s] : xf[0][s];
        aV = MFMA(b, wkv, aV); aK = MFMA(wkv, b, aK);
#pragma unroll
        for (int e = 0; e < 4; ++e) c2 = dot2(wkv[e], 0x3f803f80u, c2);
        if (tp == 2 && fp == 0) st16k(rs, wkv, lo, WS_WQKV + (16 * KS_D + 8 * w + s) * 1024);
      }
    }
#pragma unroll
    for (int b = 0; b < 2; ++b)
#pragma unroll
      for (int s = 0; s < 8; ++s)
#pragma unroll
        for (int e = 0; e < 4; ++e) { s1[b] = dot2(xf[b][s][e], 0x3f803f80u, s1[b]); s2[b] = dot2(xf[b][s][e], xf[b][s][e], s2[b]); }
    LAS char* xa = L + K1_X + w * 16384 + lo;
    LAS char* wa = L + K1_W + w * 16384;
    {
      const lcp wp = (lcp)L + K1_W + w * 16384 + (4 * h + trq) * 128 + (16 * trcb + 4 * trp) * 2;
      f32x16 acc[4] = {};
      float c0 = 0.f, c1 = 0.f;
      asm volatile("s_waitcnt vmcnt(8)" ::: "memory");
#pragma unroll
      for (int s = 0; s < 4; ++s) { wf[0][s] = tr_frag(wp, 128, s); wf[1][s] = tr_frag(wp + 64, 128, s); }
      asm volatile("s_waitcnt vmcnt(0)" ::: "memory");
#pragma unroll
      for (int s = 4; s < 8; ++s) { wf[0][s] = tr_frag(wp, 128, s); wf[1][s] = tr_frag(wp + 64, 128, s); }
      SBAR();
      if (kv) {
        put4(aV, wa + lo, 4); put4(aK, wa + 4096 + lo, 2);
        c2 = xsum(c2);
        if (h == 0) *(LAS float*)(wa + 6144 + 256 + r * 4) = c2;
      }
#pragma unroll
      for (int q = 0; q < 4; ++q) {
#pragma unroll
        for (int s = 0; s < 8; ++s) {
          acc[q] = MFMA(wf[q >> 1][s], xf[q & 1][s], acc[q]);
          if (q == 0) { c0 = dot2(wf[0][s][0], 0x3f803f80u, c0); c0 = dot2(wf[0][s][1], 0x3f803f80u, c0); c0 = dot2(wf[0][s][2], 0x3f803f80u, c0); c0 = dot2(wf[0][s][3], 0x3f803f80u, c0); }
          if (q == 1) { c1 = dot2(wf[1][s][0], 0x3f803f80u, c1); c1 = dot2(wf[1][s][1], 0x3f803f80u, c1); c1 = dot2(wf[1][s][2], 0x3f803f80u, c1); c1 = dot2(wf[1][s][3], 0x3f803f80u, c1); }
          if (q > 0 && s == 1) put4(acc[q - 1], xa + (q - 1) * 4096, 4);
          SBAR();
        }
      }
      put4(acc[3], xa + 3 * 4096, 4);
      c0 = xsum(c0); c1 = xsum(c1);
      *(LAS float*)(wa + 6144 + h * 128 + r * 4) = h ? c1 : c0;
    }
#pragma unroll
    for (int b = 0; b < 2; ++b) { const f32x2 sv = {s1[b], s2[b]}; *(LAS f32x2*)(wa + 7168 + b * 512 + lane * 8) = sv; }
    if (tp == 1) {
#pragma unroll
      for (int s = 0; s < 8; ++s) {
        st16k(rs, wf[0][s], lo, WS_WQKV + ((2 * fp) * KS_D + 8 * w + s) * 1024);
        st16k(rs, wf[1][s], lo, WS_WQKV + ((2 * fp + 1) * KS_D + 8 * w + s) * 1024);
      }
    }
  } else {
    unsigned warm = 0;
    {
      unsigned clo, chi;
      if (w == 0) { CODE_ADDR(first_kernel, clo, chi); } else { CODE_ADDR(main_kernel, clo, chi); }
      const char* cp = (const char*)(((unsigned long long)chi << 32) | clo);
      const int off = (w == 0 ? 0 : (w - 1) * 8192) + lane * 128;
      asm volatile("global_load_dword %0, %1, off" : "=v"(warm) : "v"(cp + off) : "memory");
      if ((w == 0 && off + 8192 < FIRST_CODE_BYTES) || (w == 3 && off + 8192 < MAIN_CODE_BYTES))
        asm volatile("global_load_dword %0, %1, off" : "+v"(warm) : "v"(cp + off + 8192) : "memory");
    }
    const unsigned short* src = w1; int ldw = DFF, k0 = 0, c0 = 0, dst = 0, ksn = KS_D;
    if (jw < 56) { src = w1; ldw = DFF; k0 = 256 * (jw & 1) + 64 * w; c0 = 64 * (jw >> 1); dst = WS_W1; ksn = KS_D; }
    else if (jw < 72) { const int b = (jw - 56) * 4 + w; src = wo; ldw = 512; k0 = 64 * (b & 7); c0 = 64 * (b >> 3); dst = WS_WO; ksn = KS_D; }
    else { const int b = (jw - 72) * 4 + w; src = w2; ldw = 512; k0 = 64 * (b >> 3); c0 = 64 * (b & 7); dst = WS_W2; ksn = KS_F; }
    if (jw < 128) {
      const unsigned short* p = src + (size_t)(k0 + (lane >> 3)) * ldw + c0 + (lane & 7) * 8;
#pragma unroll
      for (int i = 0; i < 8; ++i) glds16(p + (size_t)i * 8 * ldw, lds0 + K1_S + w * 8192 + i * 1024);
      asm volatile("s_waitcnt vmcnt(0)" ::: "memory");
      f32x16 c0a = {}, c1a = {};
      const lcp wp = (lcp)L + K1_S + w * 8192 + (4 * h + trq) * 128 + (16 * trcb + 4 * trp) * 2;
      const int d0 = dst + ((c0 >> 5) * ksn + (k0 >> 4)) * 1024;
      u32x4 a[4], b[4];
#pragma unroll
      for (int s = 0; s < 4; ++s) { a[s] = tr_frag(wp, 128, s); b[s] = tr_frag(wp + 64, 128, s); }
#pragma unroll
      for (int s = 0; s < 4; ++s) { st16k(rs, a[s], lo + s * 1024, d0); st16k(rs, b[s], lo + s * 1024, d0 + ksn * 1024); }
      if (jw < 56) {
#pragma unroll
        for (int s = 0; s < 4; ++s) { c0a = MFMA(a[s], ones, c0a); c1a = MFMA(b[s], ones, c1a); }
        if (r == 0) {
          LAS char* cp = L + K1_S + w * 8192 + h * 16;
#pragma unroll
          for (int g = 0; g < 4; ++g) {
            f32x4 u = {c0a[4 * g], c0a[4 * g + 1], c0a[4 * g + 2], c0a[4 * g + 3]}, v = {c1a[4 * g], c1a[4 * g + 1], c1a[4 * g + 2], c1a[4 * g + 3]};
            *(LAS f32x4*)(cp + g * 32) = u; *(LAS f32x4*)(cp + 128 + g * 32) = v;
          }
        }
      }
    }
    asm volatile("s_waitcnt vmcnt(0)" :: "v"(warm) : "memory");
    {
      const u32x4 sv = {SENT, SENT, SENT, SENT};
      const int gw = blockIdx.x * 4 + w;
      st16k(rs, sv, lo, WS_O + gw * 2048); st16k(rs, sv, lo + 1024, WS_O + gw * 2048);
      if (gw < 128) st16k(rs, sv, lo, WS_KV + KV_STR + gw * 1024);
    }
  }
  BAR();
  const int kq = w8 >> 1, ti = w8 & 1;
  float mean, rstd;
  f32x4 ps[2][4], pc[2][4];
  {
    f32x2 sv[4];
#pragma unroll
    for (int sw = 0; sw < 4; ++sw) sv[sw] = *(const LAS f32x2*)(L + K1_W + sw * 16384 + 7168 + ti * 512 + lane * 8);
#pragma unroll
    for (int g = 0; g < 2; ++g)
#pragma unroll
      for (int sw = 0; sw < 4; ++sw) {
        ps[g][sw] = *(const LAS f32x4*)(L + K1_X + sw * 16384 + (2 * (kq >> 1) + ti) * 4096 + (2 * (kq & 1) + g) * 1024 + lo);
        pc[g][sw] = *(const LAS f32x4*)(L + K1_W + sw * 16384 + 6144 + (kq >> 1) * 128 + (2 * (kq & 1) + g) * 32 + h * 16);
      }
    SBAR();
    float s1 = (sv[0][0] + sv[1][0]) + (sv[2][0] + sv[3][0]), s2 = (sv[0][1] + sv[1][1]) + (sv[2][1] + sv[3][1]);
    s1 = xsum(s1); s2 = xsum(s2);
    mean = s1 * (1.0f / 512.0f);
    float var = __builtin_fmaf(-mean, mean, s2 * (1.0f / 512.0f));
    if (__builtin_expect(ANY(!(mean * mean <= 64.0f * var)), 0)) {
      const unsigned short* xp = x + (size_t)(64 * tp + 32 * ti + r) * 512 + 256 * h;
      float m2 = 0.f;
      for (int i = 0; i < 32; ++i) {
        const u32x4 v = *(const u32x4*)(xp + i * 8);
#pragma unroll
        for (int e = 0; e < 4; ++e) { const float a = bflo(v[e]) - mean, b = bfhi(v[e]) - mean; m2 = __builtin_fmaf(a, a, m2); m2 = __builtin_fmaf(b, b, m2); }
      }
      var = xsum(m2) * (1.0f / 512.0f);
    }
    rstd = __builtin_amdgcn_rsqf(var + 1e-5f);
  }
  if (kv && ti == fp && kq < 3) {
    const int kvo = WS_KV + (32 * fp + tp) * 2048;
    if (kq == 2) {
      float k[8];
      f32x4 ks[2][4], kc[2][4];
#pragma unroll
      for (int g = 0; g < 2; ++g)
#pragma unroll
        for (int sw = 0; sw < 4; ++sw) { ks[g][sw] = *(const LAS f32x4*)(L + K1_W + sw * 16384 + 4096 + g * 1024 + lo); kc[g][sw] = *(const LAS f32x4*)(L + K1_W + sw * 16384 + 6144 + 256 + g * 32 + h * 16); }
      SBAR();
#pragma unroll
      for (int g = 0; g < 2; ++g) {
        const f32x4 s = (ks[g][0] + ks[g][1]) + (ks[g][2] + ks[g][3]), c = (kc[g][0] + kc[g][1]) + (kc[g][2] + kc[g][3]);
#pragma unroll
        for (int e = 0; e < 4; ++e) k[4 * g + e] = __builtin_fmaf(-mean, c[e], s[e]) * rstd;
      }
      const u32x4 ko = {clean(pk2(k[0], k[1])), clean(pk2(k[2], k[3])), clean(pk2(k[4], k[5])), clean(pk2(k[6], k[7]))};
      st16k(rs, ko, lo, kvo);
    } else {
      LAS char* tb = L + K1_S + kq * 512;
      if (h == 0) { const f32x2 m = {mean, rstd}; *(LAS f32x2*)(tb + r * 8) = m; }
      float csv = 0.f;
#pragma unroll
      for (int sw = 0; sw < 4; ++sw) csv += *(const LAS float*)(L + K1_W + sw * 16384 + 6144 + 256 + r * 4);
      if (tp == 2 && fp == 0 && kq == 0 && h == 0) __builtin_amdgcn_raw_buffer_store_b32(F2U(csv), rs, r * 4, WS_CSQKV + 2048, K1_AUX);
      float v[8];
      f32x4 vs[2][4];
#pragma unroll
      for (int g2 = 0; g2 < 2; ++g2)
#pragma unroll
        for (int sw = 0; sw < 4; ++sw) vs[g2][sw] = *(const LAS f32x4*)(L + K1_W + sw * 16384 + (2 * kq + g2) * 1024 + lo);
      SBAR();
#pragma unroll
      for (int g2 = 0; g2 < 2; ++g2) {
        const f32x4 s = (vs[g2][0] + vs[g2][1]) + (vs[g2][2] + vs[g2][3]);
        const lcp mp = (lcp)tb + (16 * kq + 8 * g2 + 4 * h) * 8;
        const f32x4 m01 = *(const LAS f32x4*)mp, m23 = *(const LAS f32x4*)(mp + 16);
        v[4 * g2] = __builtin_fmaf(-m01[0], csv, s[0]) * m01[1]; v[4 * g2 + 1] = __builtin_fmaf(-m01[2], csv, s[1]) * m01[3];
        v[4 * g2 + 2] = __builtin_fmaf(-m23[0], csv, s[2]) * m23[1]; v[4 * g2 + 3] = __builtin_fmaf(-m23[2], csv, s[3]) * m23[3];
      }
      const u32x4 vo = {clean(pk2(v[0], v[1])), clean(pk2(v[2], v[3])), clean(pk2(v[4], v[5])), clean(pk2(v[6], v[7]))};
      st16k(rs, vo, r >= 16 ? 1024 + kq * 512 + h * 256 + (r - 16) * 16 : 0x30000000, kvo);
    }
  }
  {
    u32x4 qf;
    const float sc = rstd * QSCALE;
#pragma unroll
    for (int g = 0; g < 2; ++g) {
      const f32x4 s = (ps[g][0] + ps[g][1]) + (ps[g][2] + ps[g][3]), c = (pc[g][0] + pc[g][1]) + (pc[g][2] + pc[g][3]);
      qf[2 * g] = pk2(__builtin_fmaf(-mean, c[0], s[0]) * sc, __builtin_fmaf(-mean, c[1], s[1]) * sc);
      qf[2 * g + 1] = pk2(__builtin_fmaf(-mean, c[2], s[2]) * sc, __builtin_fmaf(-mean, c[3], s[3]) * sc);
      if (tp == 1 && ti == 0 && r == 0) st16k(rs, __builtin_bit_cast(u32x4, c), h * 16 + g * 32, WS_CSQKV + 64 * (4 * fp + kq));
    }
    st16k(rs, qf, lo, WS_Q + ((2 * tp + ti) * KS_D + 4 * fp + kq) * 1024);
  }
  if (jw < 56 && w8 == 7) {
    const LAS float* cp = (const LAS float*)(L + K1_S) + lane;
    const float s = (cp[0] + cp[2048]) + (cp[4096] + cp[6144]);
    __builtin_amdgcn_raw_buffer_store_b32(F2U(s), rs, lane * 4, WS_CS1 + (jw & 1) * 8192 + (jw >> 1) * 256, K1_AUX);
  }
}

DI lcp opaque(lcp p) { unsigned a = (unsigned)(uintptr_t)p; asm volatile("" : "+v"(a)); return (lcp)(uintptr_t)a; }
constexpr int M_A0 = 0, M_B0 = 32768, M_A1 = 65536, M_B1 = 98304;
constexpr int M_MS = 131 * 1024;
constexpr int M_WOT = 132 * 1024;
constexpr int M_W2T = 140 * 1024;
constexpr int M_CS = 156 * 1024;
constexpr int MAIN_LDS = 160 * 1024;
#define UNDEF(x) asm volatile("" : "=v"(x))
static_assert(M_MS >= KIMG_BYTES + 64 * VSTR, "K/V image must fit below the tables");
#define EX(x) __builtin_amdgcn_exp2f(x)

DI void attn_simple(rsrc_t rs, int kvb, const u32x4 qf, f32x16& o, int lane) {
  const int r = lane & 31, h = lane >> 5;
  float tm = -INFINITY;
  for (int t = 0; t < 64; ++t) {
    const u32x4 kx = ld16c(rs, lane * 16, kvb + t * 2048);
    f32x16 z = {};
    f32x16 s = MFMA(kx, qf, z);
#pragma unroll
    for (int i = 0; i < 16; ++i) tm = __builtin_fmaxf(tm, s[i]);
  }
  tm = xmax(tm);
  f32x16 negm;
#pragma unroll
  for (int i = 0; i < 16; ++i) { negm[i] = -tm; o[i] = 0.f; }
  const unsigned cr = r == 16 ? 0x3f803f80u : 0u;
  const u32x4 cv = {cr, cr, cr, cr};
  for (int t = 0; t < 64; ++t) {
    const u32x4 kf = ld16c(rs, lane * 16, kvb + t * 2048);
    u32x4 v0 = ld16c(rs, 1024 + h * 256 + (r & 15) * 16, kvb + t * 2048);
    u32x4 v1 = ld16c(rs, 1536 + h * 256 + (r & 15) * 16, kvb + t * 2048);
    if (r >= 16) { v0 = cv; v1 = cv; }
    f32x16 s = MFMA(kf, qf, negm);
#pragma unroll
    for (int i = 0; i < 16; ++i) s[i] = EX(s[i]);
    u32x4 p0 = {pk2(s[0], s[1]), pk2(s[2], s[3]), pk2(s[4], s[5]), pk2(s[6], s[7])};
    u32x4 p1 = {pk2(s[8], s[9]), pk2(s[10], s[11]), pk2(s[12], s[13]), pk2(s[14], s[15])};
    o = MFMA(v0, p0, o);
    o = MFMA(v1, p1, o);
  }
}

DI void put_tile(const f32x16& a, LAS char* p, int owner_stride) {
#pragma unroll
  for (int g = 0; g < 4; ++g) {
    f32x4 v = {a[4 * g], a[4 * g + 1], a[4 * g + 2], a[4 * g + 3]};
    *(LAS f32x4*)(p + (g >> 1) * owner_stride + (g & 1) * 1024) = v;
  }
}
template <int N> DI void get_half(float (&o)[8], lcp p) {
  f32x4 a = {o[0], o[1], o[2], o[3]}, c = {o[4], o[5], o[6], o[7]};
#pragma unroll
  for (int b = 0; b < N; b += 4) {
    f32x4 v[4][2];
#pragma unroll
    for (int s = 0; s < 4; ++s) { v[s][0] = *(const LAS f32x4*)(p + (b + s) * 2048); v[s][1] = *(const LAS f32x4*)(p + (b + s) * 2048 + 1024); }
    SBAR();
#pragma unroll
    for (int s = 0; s < 4; ++s) { a += v[s][0]; c += v[s][1]; }
    if (b + 4 < N) asm volatile("" : "+v"(a), "+v"(c) :: "memory");
  }
#pragma unroll
  for (int e = 0; e < 4; ++e) { o[e] = a[e]; o[4 + e] = c[e]; }
}
DI u32x4 publish_x(rsrc_t rs, const float (&o)[8], int xoff, int stoff, int lane) {
  const int r = lane & 31, h = lane >> 5;
  const u32x4 y = {pk2(o[0], o[1]), pk2(o[2], o[3]), pk2(o[4], o[5]), pk2(o[6], o[7])};
  st16c(rs, y, lane * 16, xoff);
  SBAR();
  float v[8];
#pragma unroll
  for (int i = 0; i < 4; ++i) { v[2 * i] = bflo(y[i]); v[2 * i + 1] = bfhi(y[i]); }
  float s = ((v[0] + v[1]) + (v[2] + v[3])) + ((v[4] + v[5]) + (v[6] + v[7]));
  s = xsum(s);
  const float mu = s * (1.0f / 16.0f);
  float m2 = 0.f;
#pragma unroll
  for (int i = 0; i < 8; ++i) { const float d = v[i] - mu; m2 = __builtin_fmaf(d, d, m2); }
  m2 = xsum(m2);
  const f32x2 st = {s, m2};
  st8c(rs, st, h ? 0x30000000 : r * 16, stoff);
  return y;
}
template <int CTRL> DI float dpp(float v) { return __int_as_float(__builtin_amdgcn_update_dpp(0, __float_as_int(v), CTRL, 0xf, 0xf, true)); }
DI float sum8(float v) { v += dpp<0xB1>(v); v += dpp<0x4E>(v); v += dpp<0x141>(v); return v; }
DI f32x2 combine4(const u32x4 a, const u32x4 b) {
  const float s0 = U2F(a[0]), s1 = U2F(a[2]), s2 = U2F(b[0]), s3 = U2F(b[2]);
  const float mean = sum8((s0 + s1) + (s2 + s3)) * (1.0f / 512.0f);
  const float d0 = __builtin_fmaf(s0, 1.0f / 16.0f, -mean), d1 = __builtin_fmaf(s1, 1.0f / 16.0f, -mean);
  const float d2 = __builtin_fmaf(s2, 1.0f / 16.0f, -mean), d3 = __builtin_fmaf(s3, 1.0f / 16.0f, -mean);
  const float dd = (d0 * d0 + d1 * d1) + (d2 * d2 + d3 * d3);
  const float m2 = (U2F(a[1]) + U2F(a[3])) + (U2F(b[1]) + U2F(b[3]));
  const float tot = sum8(__builtin_fmaf(dd, 16.0f, m2));
  f32x2 r = {mean, __builtin_amdgcn_rsqf(tot * (1.0f / 512.0f) + 1e-5f)};
  return r;
}

#define D_O 40
#define D_X1 12
#define D_X2 10
#define D_KV 4
#define H1S 7
#define XS1 16
#define D_O1 16
#define PRIO1 1
#define NW2L 12
#define NW2G 8
#define H0S 10
#define DELAY(n) do { if ((n) > 0) __builtin_amdgcn_s_sleep(n); } while (0)

extern "C" __global__ void NOPK __launch_bounds__(512, 2) main_kernel(char* ws, const unsigned short* xin, unsigned short* out) {
  extern __shared__ __attribute__((aligned(16))) char smem[];
  LAS char* L = (LAS char*)smem;
  const rsrc_t rs = ws_rsrc(ws);
  const unsigned lds0 = (unsigned)(uintptr_t)smem;
  const int w = __builtin_amdgcn_readfirstlane(threadIdx.x >> 6);
  const int xcd = blockIdx.x & 7, idx = blockIdx.x >> 3;
  const int tp = 4 * xcd + (idx >> 3), fp = idx & 7;
  const int kq = w & 3, ti = w >> 2;
  const int tt = 2 * tp + ti, og = 4 * fp + kq;
  int lane = threadIdx.x & 63, r = lane & 31, h = lane >> 5, lo = lane * 16;
#define RELANE() do { lane = __builtin_amdgcn_mbcnt_hi(~0u, __builtin_amdgcn_mbcnt_lo(~0u, 0u)); asm volatile("" : "+v"(lane)); lane &= 63; r = lane & 31; h = lane >> 5; lo = lane * 16; } while (0)
  const int own = (tt * KS_D + og) * 1024;
  const int ownst = tt * 8192 + (og >> 1) * 512 + (og & 1) * 8;
  const int ft1 = 7 * fp + (w < 7 ? w : 6);
  const int w1o = w < 7 ? WS_W1 + ft1 * KS_D * 1024 : 0x40000000;
  const int woo = WS_WO + ((2 * fp) * KS_D + 8 * kq) * 1024;
  const int w2o = WS_W2 + ((2 * fp) * KS_F + 14 * w) * 1024;
  const int hown = w < 7 ? WS_H + ((2 * tp) * KS_F + 2 * ft1) * 1024 : 0x20000000;
  const int rot = 4 * xcd;

  u32x4 ka[4];
#pragma unroll
  for (int i = 0; i < 4; ++i) ka[i] = LDKC(WS_KV + ((2 * w + rot) & 31) * 2048, i);
  u32x4 qf = ld16(rs, lo, WS_Q + own);
  u32x4 res;
  {
    const unsigned short* xp = xin + (size_t)(32 * tt + r) * D_ + 16 * og + 4 * h;
    const u32x2 a0 = *(const u32x2*)xp, a1 = *(const u32x2*)(xp + 8);
    res = (u32x4){a0[0], a0[1], a1[0], a1[1]};
  }

#pragma unroll 1
  for (int layer = 0; layer < 2; ++layer) {
    RELANE();
    u32x4 w1f[32], w2[28];
#define W2LOAD(n) w2[n] = LDK(sop(w2o + ((n) & 1) * KS_F * 1024), (n) >> 1)
#define DMA(goff, loff) glds16(ws + (size_t)(unsigned)sop(goff) + lo, lds0 + (loff))
    {
      const int kvb = WS_KV + layer * KV_STR;
      const int kva_ = sop(kvb + ((2 * w + rot) & 31) * 2048), kvb_ = sop(kvb + ((16 + 2 * w + rot) & 31) * 2048);
      u32x4 kb[4], kc[8];
#define KVA(i) LDKC(kva_, i)
#define KVB(i) LDKC(kvb_, i)
#define KVC(i) LDKC(sop(kvb + (32 + ((4 * w + rot) & 31)) * 2048), i)
      if (lane < 16) {
        const unsigned c = (lane & 1) ? 0u : 0x3f803f80u;
        const u32x4 cv = {c, c, c, c};
        *(LAS u32x4*)(L + KIMG_BYTES + (8 * w + (lane >> 1)) * VSTR + 1024 + (lane & 1) * 16) = cv;
      }
      LAS char* kdst = L + lo;
      LAS char* vdst = L + KIMG_BYTES + lo;
#define KVPUT(src, i, pos) do { if ((i) & 1) *(LAS u32x4*)(vdst + ((pos) + ((i) >> 1)) * VSTR) = src[i]; else *(LAS u32x4*)(kdst + ((pos) + ((i) >> 1)) * 1024) = src[i]; } while (0)
      VERIFY(0, 4, ka, KVA(i));
#pragma unroll
      for (int i = 0; i < 4; ++i) KVPUT(ka, i, 2 * w);
#pragma unroll
      for (int i = 0; i < 4; ++i) kb[i] = KVB(i);
      BAR();
      lcp kq_ = opaque((lcp)L + lo);
      lcp vq0 = opaque((lcp)L + KIMG_BYTES + (r < 16 ? h * 256 + r * 16 : (r > 16) ? 1040 : 1024));
      lcp vq1 = opaque((lcp)L + KIMG_BYTES + (r < 16 ? 512 + h * 256 + r * 16 : (r > 16) ? 1040 : 1024));
      f32x16 negm, o;
      u32x4 kf = *(lds_u4p)kq_;
      {
        f32x16 z = {};
        f32x16 s = MFMA(kf, qf, z);
        float m = s[0];
#pragma unroll
        for (int i = 1; i < 16; ++i) m = __builtin_fmaxf(m, s[i]);
        m = xmax(m);
#pragma unroll
        for (int i = 0; i < 16; ++i) { negm[i] = -m; o[i] = 0.f; }
        PIN(negm);
      }
      f32x16 sa = MFMA(kf, qf, negm), sb;
      kf = *(lds_u4p)(kq_ + 1024);
      u32x4 v0 = *(lds_u4p)vq0;
      u32x4 p1 = {0, 0, 0, 0}, v1p = v0;
      unsigned sv1;
      asm volatile("v_mov_b32 %0, -1" : "=v"(sv1));
      const u32x4 sv4 = {sv1, sv1, sv1, sv1};
      const u32x2 sv2 = {sv1, sv1};
      const int fz = layer == 0 ? 0 : 0x40000000;
#define FILL(off, k) do { __builtin_amdgcn_raw_buffer_store_b128(sv4, rs, lo + (k) * 1024, sop(off), AUX_SC1); ST_PAD(sv4); } while (0)
#define FILLST(off) __builtin_amdgcn_raw_buffer_store_b64(sv2, rs, h ? 0x30000000 : r * 16, sop((off) + ownst), AUX_SC1)
      auto side = [&](auto NC) AI {
        constexpr int n = decltype(NC)::value;
        if constexpr (n == 0) FILL(WS_X1 + own, 0);
        else if constexpr (n < 5) { constexpr int j = n - 1; FILL(hown + (j >> 1) * KS_F * 1024, j & 1); }
        else if constexpr (n == 5) FILLST(WS_ST1);
        else if constexpr (n == 6) FILL(fz + WS_X2 + own, 0);
        else if constexpr (n == 7) FILL(fz + WS_O + MB2 + own, 0);
        else if constexpr (n == 8) FILLST(fz + WS_ST2);
        else if constexpr (n < 12) { }
        else if constexpr (n < 16) KVPUT(kb, n - 12, 16 + 2 * w);
        else if constexpr (n < 24) kc[n - 16] = KVC(n - 16);
        else if constexpr (n < 40) w1f[n - 24] = LDK(sop(w1o), n - 24);
        else if constexpr (n < 48) KVPUT(kc, n - 40, 32 + 4 * w);
        else if constexpr (n < 64) { constexpr int j = (n - 48) & 7, f = (n - 48) >> 3;
          DMA(woo + (f * KS_D + j) * 1024, (kq == 3 && j >= 4) ? M_WOT + (f * 4 + j - 4) * 1024 : f * M_A1 + (kq * 8 + j) * 1024); }
        else if constexpr (n < 66) DMA(w2o + (n - 64) * KS_F * 1024 + 13 * 1024, M_W2T + (w * 2 + n - 64) * 1024);
        else if constexpr (n < 82) w1f[n - 50] = LDK(sop(w1o), n - 50);
      };
#define SIDE(n) do { if constexpr ((n) >= 0) side(std::integral_constant<int, (n) < 0 ? 0 : (n)>{}); } while (0)
#define STEP2(sc, sn, j, N) do { u32x4 p0; \
    sc[0] = EX(sc[0]); sc[1] = EX(sc[1]); sc[2] = EX(sc[2]); sc[3] = EX(sc[3]); sc[4] = EX(sc[4]); SBAR(); \
    sn = MFMA(kf, qf, negm); SBAR(); \
    kf = *(lds_u4p)(kq_ + ((j) + 2) * 1024); SIDE(N); SBAR(); \
    sc[5] = EX(sc[5]); sc[6] = EX(sc[6]); sc[7] = EX(sc[7]); sc[8] = EX(sc[8]); sc[9] = EX(sc[9]); SBAR(); \
    o = MFMA(v1p, p1, o); SBAR(); \
    v1p = *(lds_u4p)(vq1 + (j) * VSTR); SBAR(); \
    p0[0] = pk2(sc[0], sc[1]); p0[1] = pk2(sc[2], sc[3]); p0[2] = pk2(sc[4], sc[5]); p0[3] = pk2(sc[6], sc[7]); SBAR(); \
    sc[10] = EX(sc[10]); sc[11] = EX(sc[11]); sc[12] = EX(sc[12]); sc[13] = EX(sc[13]); sc[14] = EX(sc[14]); sc[15] = EX(sc[15]); SBAR(); \
    o = MFMA(v0, p0, o); SBAR(); \
    v0 = *(lds_u4p)(vq0 + ((j) + 1) * VSTR); SIDE((N) < 0 ? -1 : (N) + 1); SBAR(); \
    p1[0] = pk2(sc[8], sc[9]); p1[1] = pk2(sc[10], sc[11]); p1[2] = pk2(sc[12], sc[13]); p1[3] = pk2(sc[14], sc[15]); SBAR(); } while (0)
#define STEP4(N) do { \
      STEP2(sa, sb, 0, N); \
      STEP2(sb, sa, 1, (N) < 0 ? -1 : (N) + 2); \
      STEP2(sa, sb, 2, (N) < 0 ? -1 : (N) + 4); \
      STEP2(sb, sa, 3, (N) < 0 ? -1 : (N) + 6); \
      PIN(o); PIN(sa); PIN(p1); \
      kq_ += 4096; vq0 += 4 * VSTR; vq1 += 4 * VSTR; } while (0)
      STEP4(0);
      VERIFY(1, 4, kb, KVB(i));
      STEP4(8);
      STEP4(16);
      BAR();
      STEP4(24); STEP4(32);
      VERIFY(8, 8, kc, KVC(i));
      STEP4(40);
      STEP4(-1);
      BAR();
      if (ti == 0) { STEP4(48); STEP4(56); } else { STEP4(-1); STEP4(-1); }
      STEP4(64); STEP4(72); STEP4(80);
#pragma unroll 1
      for (int t = 48; t < 64; t += 4) STEP4(-1);
      o = MFMA(v1p, p1, o);
      float l = lowhalf(o[8]);
      bool bad = !(l < 1.0e30f) || !(l > 1.0e-30f);
#pragma unroll
      for (int i = 0; i < 8; ++i) bad |= !(__builtin_fabsf(o[i]) < 3.0e38f);
      if (__builtin_expect(ANY(bad), 0)) {
        attn_simple(rs, kvb, qf, o, lane);
        l = lowhalf(o[8]);
      }
      const float rl = __builtin_amdgcn_rcpf(l);
      const u32x4 oa = {pk2(o[0] * rl, o[1] * rl), pk2(o[2] * rl, o[3] * rl), pk2(o[4] * rl, o[5] * rl), pk2(o[6] * rl, o[7] * rl)};
      asm volatile("s_waitcnt vmcnt(0)" ::: "memory");
      st16c(rs, oa, lo, sop(WS_O + layer * MB2 + own));
      SBAR();
    }
    RELANE();
    const int bt = ti ? M_B1 : M_B0;
    const int xo_ = WS_X1 + (tt * KS_D + 8 * kq) * 1024, so_ = WS_ST1 + tt * 8192 + 8 * kq * 16;
#define XSLOAD(EN, i) ((i) < 8 ? LDKC(sop((EN) ? xo_ : 0x40000000), i) : ld16c(rs, (lane & 7) * 1024 + (lane >> 3) * 16 + ((i) - 8) * 512, sop((EN) ? so_ : 0x40000000)))
    {
      auto oproj = [&](f32x16& a0, f32x16& a1, bool mid_barrier) AI {
        u32x4 af[8], wo0[8], wo1[8];
        const lcp wl0 = (lcp)L + 8 * kq * 1024 + lo, wh0 = (lcp)L + (kq == 3 ? M_WOT : (8 * kq + 4) * 1024) + lo;
        const lcp wl1 = opaque(wl0 + M_A1), wh1 = opaque(wh0 + (kq == 3 ? 4096 : M_A1));
#pragma unroll
        for (int i = 0; i < 8; ++i) wo0[i] = *(lds_u4p)((i < 4 ? wl0 : wh0) + (i & 3) * 1024);
#pragma unroll
        for (int i = 0; i < 2; ++i) wo1[i] = *(lds_u4p)(wl1 + i * 1024);
#pragma unroll
        for (int i = 0; i < 8; ++i) PIN(wo0[i]);
        PIN(wo1[0]); PIN(wo1[1]);
        if (mid_barrier) { BAR(); DELAY(D_O1); } else DELAY(D_O);
        const int aa_ = sop(WS_O + layer * MB2 + (tt * KS_D + 8 * kq) * 1024);
        HANDOFF_LOAD(2, 8, af, LDKC(aa_, i), fold4(m_, af[i]));
#pragma unroll
        for (int i = 0; i < 8; ++i) {
          a0 = MFMA(wo0[i], af[i], a0);
          if (i < 6) wo1[2 + i] = *(lds_u4p)((i < 2 ? wl1 : wh1) + ((2 + i) & 3) * 1024);
          SBAR();
        }
#pragma unroll
        for (int i = 0; i < 8; ++i) { a1 = MFMA(wo1[i], af[i], a1); SBAR(); }
      };
      auto puts = [&](const f32x16& a0, const f32x16& a1) AI {
        put_tile(a0, L + bt + kq * 2048 + lo, 8192);
        put_tile(a1, L + bt + 16384 + kq * 2048 + lo, 8192);
      };
      auto fin = [&]() AI {
        float o[8];
#pragma unroll
        for (int i = 0; i < 4; ++i) { o[2 * i] = bflo(res[i]); o[2 * i + 1] = bfhi(res[i]); }
        get_half<4>(o, (lcp)L + bt + kq * 8192 + lo);
        (void)publish_x(rs, o, sop(WS_X1 + own), sop(WS_ST1 + ownst), lane);
      };
      if (ti == 0) {
        if (layer == 0) {
#pragma unroll
          for (int u = 0; u < 2; ++u) {
            const int t = u * 256 + w * 64 + lane;
            const int o1 = t < 224 ? WS_CS1 + (224 * fp + t) * 4 : t < 288 ? WS_CSQKV + (64 * fp + t - 224) * 4 : WS_CSQKV + (512 + t - 288) * 4;
            float c = __builtin_bit_cast(float, __builtin_amdgcn_raw_buffer_load_b32(rs, o1, 0, 0));
            c += __builtin_bit_cast(float, __builtin_amdgcn_raw_buffer_load_b32(rs, t < 224 ? o1 + 8192 : 0x40000000, 0, 0));
            if (t < 320) *(LAS float*)(L + M_CS + t * 4) = c;
          }
        }
        f32x16 a0 = {}, a1 = {};
        oproj(a0, a1, false);
        PIN(a0); PIN(a1);
#pragma unroll
        for (int n = 0; n < 12; ++n) W2LOAD(n);
        BAR();
        puts(a0, a1);
        BAR();
        fin();
        DELAY(D_X1);
        u32x4 xs[10];
        HANDOFF_LOAD(3, 10, xs, XSLOAD(true, i), fold4(m_, xs[i]));
#pragma unroll
        for (int i = 0; i < 8; ++i) *(LAS u32x4*)(L + bt + (8 * kq + i) * 1024 + lo) = xs[i];
        const f32x2 ms = combine4(xs[8], xs[9]);
        if ((lane & 7) == 0) *(LAS f32x2*)(L + M_MS + (8 * w + (lane >> 3)) * 8) = ms;
        BAR();
#pragma unroll
        for (int j = 0; j < NW2L; ++j) DMA(w2o + 56 * 1024 + (j & 1) * KS_F * 1024 + (j >> 1) * 1024, (j >> 3) * M_A1 + (kq * 8 + (j & 7)) * 1024);
      } else {
        BAR();
        f32x16 a0 = {}, a1 = {};
        oproj(a0, a1, true);
        puts(a0, a1);
        __builtin_amdgcn_s_setprio(PRIO1);
        BAR();
        fin();
#pragma unroll
        for (int n = 0; n < 12; ++n) UNDEF(w2[n]);
      }
    }
    RELANE();
    u32x4 h0[14], h1[14];
    const int h0_ = WS_H + ((2 * tp) * KS_F + 14 * w) * 1024;
#define H0LOAD(i) LDKC(sop(h0_), i)
#define H1LOAD(i) LDKC(sop(h0_ + KS_F * 1024), i)
    {
      const lcp xp = (lcp)L + M_B0 + lo;
#define XF(i) (*(lds_u4p)(xp + ((i) >> 5) * 65536 + ((i) & 31) * 1024))
      auto epi = [&](const f32x16& o, f32x2 ms, int hoff) AI {
        f32x4 cs[4];
#pragma unroll
        for (int g = 0; g < 4; ++g) cs[g] = *(const LAS f32x4*)(L + M_CS + (w < 7 ? w : 0) * 128 + g * 32 + h * 16);
        SBAR();
        const float nm = -ms[0], sc = ms[1];
        u32x4 e0, e1;
#pragma unroll
        for (int g = 0; g < 4; ++g) {
          const float a = __builtin_fmaxf(__builtin_fmaf(nm, cs[g][0], o[4 * g]) * sc, 0.f), b = __builtin_fmaxf(__builtin_fmaf(nm, cs[g][1], o[4 * g + 1]) * sc, 0.f);
          const float c = __builtin_fmaxf(__builtin_fmaf(nm, cs[g][2], o[4 * g + 2]) * sc, 0.f), d = __builtin_fmaxf(__builtin_fmaf(nm, cs[g][3], o[4 * g + 3]) * sc, 0.f);
          if (g < 2) { e0[2 * g] = pk2(a, b); e0[2 * g + 1] = pk2(c, d); }
          else { e1[2 * (g - 2)] = pk2(a, b); e1[2 * (g - 2) + 1] = pk2(c, d); }
        }
        const int eo_ = sop(hoff);
        st16c(rs, e0, lo, eo_); st16c(rs, e1, lo + 1024, eo_);
      };
      constexpr int PF = 3;
      u32x4 xa[PF];
      f32x16 a0 = {};
#pragma unroll
      for (int i = 0; i < PF; ++i) xa[i] = XF(i);
#pragma unroll
      for (int i = 0; i < 32; ++i) {
        a0 = MFMA(w1f[i], xa[i % PF], a0);
        if (i + PF < 32) xa[i % PF] = XF(i + PF);
        if (i == XS1) {
          PIN(a0);
          if (ti == 1) {
#pragma unroll
            for (int j = 0; j < 10; ++j) w2[j] = XSLOAD(true, j);
          }
        }
        SBAR();
      }
      PIN(a0);
      epi(a0, *(const LAS f32x2*)(L + M_MS + r * 8), hown);
      SBAR();
#pragma unroll
      for (int n = 12; n < 12 + NW2G; ++n) W2LOAD(n);
      SBAR();
      if (ti == 1) {
        VERIFY(4, 10, w2, XSLOAD(true, i));
#pragma unroll
        for (int i = 0; i < 8; ++i) *(LAS u32x4*)(L + bt + (8 * kq + i) * 1024 + lo) = w2[i];
        const f32x2 ms = combine4(w2[8], w2[9]);
        if ((lane & 7) == 0) *(LAS f32x2*)(L + M_MS + (8 * w + (lane >> 3)) * 8) = ms;
      }
      if (ti == 0) asm volatile("s_waitcnt vmcnt(%0)" :: "n"(2 + NW2G) : "memory");
      __builtin_amdgcn_s_setprio(0);
      BAR();
      f32x16 a1 = {};
#pragma unroll
      for (int i = 0; i < PF; ++i) xa[i] = XF(32 + i);
#pragma unroll
      for (int i = 0; i < 32; ++i) {
        a1 = MFMA(w1f[i], xa[i % PF], a1);
        if (i + PF < 32) xa[i % PF] = XF(32 + i + PF);
        if (12 + NW2G + i < 26) W2LOAD(12 + NW2G + i);
        if (i >= H0S && i < H0S + 14) h0[i - H0S] = H0LOAD(i - H0S);
        SBAR();
      }
      PIN(a1);
      epi(a1, *(const LAS f32x2*)(L + M_MS + (32 + r) * 8), hown + KS_F * 1024);
      SBAR();
    }
    RELANE();
    const bool kvw = ti == fp;
    u32x4 wq[2][8], wkv[8], xq[10];
    const int wqo = WS_WQKV + ((2 * fp) * KS_D + 8 * kq) * 1024;
    const int x2o = WS_X2 + (tt * KS_D + 8 * kq) * 1024, s2o = WS_ST2 + tt * 8192 + 8 * kq * 16;
#define XQLOAD(EN, i) ((i) < 8 ? LDKC(sop((EN) ? x2o : 0x40000000), i) : ld16c(rs, (lane & 7) * 1024 + (lane >> 3) * 16 + ((i) - 8) * 512, sop((EN) ? s2o : 0x40000000)))
    {
      LAS char* rp = L + w * 2048 + lo;
      if (ti == 1) {
#pragma unroll
        for (int n = 0; n < 12; ++n) w2[n] = *(lds_u4p)((lcp)L + (n >> 3) * M_A1 + (kq * 8 + (n & 7)) * 1024 + lo);
      }
      w2[26] = *(lds_u4p)((lcp)L + M_W2T + (w * 2) * 1024 + lo);
      w2[27] = *(lds_u4p)((lcp)L + M_W2T + (w * 2 + 1) * 1024 + lo);
      const u32x4 y1 = *(lds_u4p)((lcp)L + bt + og * 1024 + lo);
      BAR();
      auto finish = [&](int base) AI {
        float o[8];
#pragma unroll
        for (int i = 0; i < 4; ++i) { o[2 * i] = bflo(y1[i]); o[2 * i + 1] = bfhi(y1[i]); }
        get_half<8>(o, (lcp)L + base + (kq >> 1) * 65536 + (kq & 1) * 16384 + lo);
        if (layer == 1) {
          const u32x2 a0 = {pk2(o[0], o[1]), pk2(o[2], o[3])}, a1 = {pk2(o[4], o[5]), pk2(o[6], o[7])};
          int l2 = lane;
          PIN(l2);
          unsigned short* yp = out + (size_t)(32 * tt + (l2 & 31)) * D_ + 16 * og + 4 * (l2 >> 5);
          *(u32x2*)(yp) = a0; *(u32x2*)(yp + 8) = a1;
        } else {
          res = publish_x(rs, o, sop(WS_X2 + own), sop(WS_ST2 + ownst), lane);
        }
      };
      VERIFY(5, 14, h0, H0LOAD(i));
      f32x16 a0 = {}, a1 = {};
#pragma unroll
      for (int i = 0; i < 14; ++i) {
        a0 = MFMA(w2[2 * i], h0[i], a0);
        a1 = MFMA(w2[2 * i + 1], h0[i], a1);
        if (i >= H1S && i < H1S + 7) { h1[2 * (i - H1S)] = H1LOAD(2 * (i - H1S)); h1[2 * (i - H1S) + 1] = H1LOAD(2 * (i - H1S) + 1); }
        SBAR();
      }
      put_tile(a0, rp + M_A0, 16384);
      put_tile(a1, rp + M_A1, 16384);
      BAR();
#pragma unroll
      for (int i = 0; i < 14; ++i) PIN(h1[i]);
      res = y1;
      if (ti == 0) finish(M_A0);
      VERIFY(6, 14, h1, H1LOAD(i));
      f32x16 b0 = {}, b1 = {};
#pragma unroll
      for (int i = 0; i < 10; ++i) {
        b0 = MFMA(w2[2 * i], h1[i], b0);
        b1 = MFMA(w2[2 * i + 1], h1[i], b1);
        if (i == 2 || i == 5) {
          PIN(b0); PIN(b1);
#pragma unroll
          for (int j = 0; j < 8; ++j) UNDEF(wq[i == 5][j]);
          if (ti == 0 && layer == 0) {
#pragma unroll
            for (int j = 0; j < 8; ++j) wq[i == 5][j] = LDK(sop(wqo + (i == 5) * KS_D * 1024), j);
          }
        }
        if (i == 8) {
          PIN(b0); PIN(b1);
#pragma unroll
          for (int j = 0; j < 8; ++j) UNDEF(wkv[j]);
          if (kvw && ti == 0 && layer == 0) {
#pragma unroll
            for (int j = 0; j < 8; ++j) wkv[j] = LDK(sop(WS_WQKV + (16 * KS_D + 8 * kq) * 1024), j);
          }
        }
        SBAR();
      }
#pragma unroll
      for (int i = 10; i < 14; ++i) { b0 = MFMA(w2[2 * i], h1[i], b0); SBAR(); }
#pragma unroll
      for (int i = 10; i < 14; ++i) {
        b1 = MFMA(w2[2 * i + 1], h1[i], b1);
        if (i == 11) put_tile(b0, rp + M_B0, 16384);
        SBAR();
      }
      PIN(b1);
#pragma unroll
      for (int j = 0; j < 10; ++j) UNDEF(xq[j]);
      if (ti == 0 && layer == 0) {
#pragma unroll
        for (int j = 0; j < 10; ++j) xq[j] = XQLOAD(true, j);
      }
      put_tile(b1, rp + M_B1, 16384);
      BAR();
      if (ti == 1) finish(M_B0);
    }
    if (layer == 1) return;
    RELANE();
    qf = res;
    if (ti == 1) {
#pragma unroll
      for (int j = 0; j < 8; ++j) { wq[0][j] = LDK(sop(wqo), j); wq[1][j] = LDK(sop(wqo + KS_D * 1024), j); }
      if (kvw) {
#pragma unroll
        for (int j = 0; j < 8; ++j) wkv[j] = LDK(sop(WS_WQKV + (16 * KS_D + 8 * kq) * 1024), j);
      }
#pragma unroll
      for (int i = 0; i < 10; ++i) xq[i] = XQLOAD(true, i);
    }
#pragma unroll
    for (int T = 0; T < 3; ++T) {
      if (T > 0 && ti != T - 1 && fp == T - 1 && kq < 3) {
        const int kvo = sop(WS_KV + KV_STR + (32 * (T - 1) + tp) * 2048);
        const lcp kp = (lcp)L + M_WOT + lo;
        if (kq == 2) {
          const f32x2 mk = *(const LAS f32x2*)(L + M_MS + (32 * (T - 1) + r) * 8);
          float k[8];
          f32x4 pv[2][4], ck[2];
#pragma unroll
          for (int g = 0; g < 2; ++g) {
            ck[g] = *(const LAS f32x4*)(L + M_CS + (288 + 8 * g + 4 * h) * 4);
#pragma unroll
            for (int sw = 0; sw < 4; ++sw) pv[g][sw] = *(const LAS f32x4*)(kp + sw * 6144 + (4 + g) * 1024);
          }
          SBAR();
#pragma unroll
          for (int g = 0; g < 2; ++g) {
            const f32x4 s = (pv[g][0] + pv[g][1]) + (pv[g][2] + pv[g][3]);
#pragma unroll
            for (int e = 0; e < 4; ++e) k[4 * g + e] = __builtin_fmaf(-mk[0], ck[g][e], s[e]) * mk[1];
          }
          const u32x4 ko = {pk2(k[0], k[1]), pk2(k[2], k[3]), pk2(k[4], k[5]), pk2(k[6], k[7])};
          st16c(rs, ko, lo, kvo);
        } else {
          float v[8];
          const float csv = *(const LAS float*)(L + M_CS + (288 + r) * 4);
          f32x4 pv[2][4], mm[2][2];
#pragma unroll
          for (int g2 = 0; g2 < 2; ++g2) {
            const lcp mp = (lcp)L + M_MS + (32 * (T - 1) + 16 * kq + 8 * g2 + 4 * h) * 8;
            mm[g2][0] = *(const LAS f32x4*)mp; mm[g2][1] = *(const LAS f32x4*)(mp + 16);
#pragma unroll
            for (int sw = 0; sw < 4; ++sw) pv[g2][sw] = *(const LAS f32x4*)(kp + sw * 6144 + (2 * kq + g2) * 1024);
          }
          SBAR();
#pragma unroll
          for (int g2 = 0; g2 < 2; ++g2) {
            const f32x4 s = (pv[g2][0] + pv[g2][1]) + (pv[g2][2] + pv[g2][3]);
            const f32x4 m01 = mm[g2][0], m23 = mm[g2][1];
            v[4 * g2] = __builtin_fmaf(-m01[0], csv, s[0]) * m01[1]; v[4 * g2 + 1] = __builtin_fmaf(-m01[2], csv, s[1]) * m01[3];
            v[4 * g2 + 2] = __builtin_fmaf(-m23[0], csv, s[2]) * m23[1]; v[4 * g2 + 3] = __builtin_fmaf(-m23[2], csv, s[3]) * m23[3];
          }
          const u32x4 vo = {pk2(v[0], v[1]), pk2(v[2], v[3]), pk2(v[4], v[5]), pk2(v[6], v[7])};
          st16c(rs, vo, r >= 16 ? 1024 + kq * 512 + h * 256 + (r - 16) * 16 : 0x30000000, kvo);
        }
      }
      if (T < 2 && ti == T) {
        VERIFY(7, 10, xq, XQLOAD(true, i));
        if (kvw) {
          f32x16 aK = {}, aV = {};
#pragma unroll
          for (int i = 0; i < 8; ++i) { aV = MFMA(xq[i], wkv[i], aV); aK = MFMA(wkv[i], xq[i], aK); }
          LAS char* kp = L + M_WOT + kq * 6144 + lo;
          put4(aV, kp, 4); put4(aK, kp + 4096, 2);
        }
        f32x16 a0 = {}, a1 = {};
#pragma unroll
        for (int i = 0; i < 8; ++i) { a0 = MFMA(wq[0][i], xq[i], a0); a1 = MFMA(wq[1][i], xq[i], a1); }
        {
          const f32x2 msw = combine4(xq[8], xq[9]);
          if ((lane & 7) == 0) *(LAS f32x2*)(L + M_MS + (8 * w + (lane >> 3)) * 8) = msw;
        }
        put_tile(a0, L + ti * M_A1 + kq * 2048 + lo, 8192);
        put_tile(a1, L + ti * M_A1 + 16384 + kq * 2048 + lo, 8192);
      }
      if (T > 0 && ti == T - 1) {
        float q[8] = {0.f, 0.f, 0.f, 0.f, 0.f, 0.f, 0.f, 0.f};
        get_half<4>(q, (lcp)L + ti * M_A1 + kq * 8192 + lo);
        const f32x2 ms = *(const LAS f32x2*)(L + M_MS + (32 * ti + r) * 8);
        const float nm = -ms[0], sc = ms[1] * QSCALE;
#pragma unroll
        for (int g = 0; g < 2; ++g) {
          const f32x4 cq = *(const LAS f32x4*)(L + M_CS + (224 + 16 * kq + 8 * g + 4 * h) * 4);
          qf[2 * g] = pk2(__builtin_fmaf(nm, cq[0], q[4 * g]) * sc, __builtin_fmaf(nm, cq[1], q[4 * g + 1]) * sc);
          qf[2 * g + 1] = pk2(__builtin_fmaf(nm, cq[2], q[4 * g + 2]) * sc, __builtin_fmaf(nm, cq[3], q[4 * g + 3]) * sc);
        }
      }
      if (T < 2) BAR();
      if (T == 1) {
        DELAY(D_KV);
#pragma unroll
        for (int i = 0; i < 4; ++i) ka[i] = LDKC(sop(WS_KV + KV_STR + ((2 * w + rot) & 31) * 2048), i);
        SBAR();
      }
    }
    BAR();
  }
}

extern "C" void kernel_launch(void* const* d_in, const int* in_sizes, int n_in, void* d_out, int out_size, void* d_ws, size_t ws_size,
                              hipStream_t stream) {
  const unsigned short* x = (const unsigned short*)d_in[0];
  const unsigned short* wq = (const unsigned short*)d_in[1];
  const unsigned short* wk = (const unsigned short*)d_in[2];
  const unsigned short* wv = (const unsigned short*)d_in[3];
  const unsigned short* wo = (const unsigned short*)d_in[4];
  const unsigned short* w1 = (const unsigned short*)d_in[6];
  const unsigned short* w2 = (const unsigned short*)d_in[7];
  char* ws = (char*)d_ws;
  (void)hipFuncSetAttribute((const void*)first_kernel, hipFuncAttributeMaxDynamicSharedMemorySize, K1_LDS);
  (void)hipFuncSetAttribute((const void*)main_kernel, hipFuncAttributeMaxDynamicSharedMemorySize, MAIN_LDS);
  first_kernel<<<256, 512, K1_LDS, stream>>>(x, wq, wk, wv, wo, w1, w2, ws);
  main_kernel<<<256, 512, MAIN_LDS, stream>>>(ws, x, (unsigned short*)d_out);
}
```
